```python
import torch

def _softmax(x, axis=-1):
    return torch.softmax(x, dim=axis)
def _gelu(x):
    return torch.nn.functional.gelu(x, approximate="tanh")
def _silu(x):
    return torch.nn.functional.silu(x)
def _sigmoid(x):
    return torch.sigmoid(x)
def _layernorm(x, axis=-1, eps=1e-5):
    m = x.mean(dim=axis, keepdim=True)
    v = x.var(dim=axis, keepdim=True, unbiased=False)
    return (x - m) / torch.sqrt(v + eps)
def _scatter_add(tgt, idx, src, axis=0):
    return tgt.clone().index_add_(axis, idx, src)
def _segment_sum(src, seg, n):
    out = torch.zeros((n,) + src.shape[1:], dtype=src.dtype)
    return out.index_add_(0, seg, src)
def _pad(x, pw):
    flat = tuple(v for pair in reversed(pw) for v in pair)
    return torch.nn.functional.pad(x, flat)

kernel_name = 'recurrent_scan__sliding_block_cached__dense_activation_squared_relu__decode'

def setup_inputs():

    _w = (lambda a: (lambda t: t.to(torch.bfloat16) if t.is_floating_point() else t)(torch.as_tensor(a)))
    def _A_setup():
        B, Sq, D = 1, 1, 640
        Hq, Hkv, Sk, Dh = 32, 32, 32768, 192
        assert Hq % Hkv == 0, (Hq, Hkv)
        return {
            "x": _w((lambda *s: torch.randn(*s, dtype=torch.bfloat16) * 0.05)(B, Sq, D)),
            "k_cache": _w((lambda *s: torch.randn(*s, dtype=torch.bfloat16) * 0.8)(B, Hkv, Sk, Dh)),
            "v_cache": _w((lambda *s: torch.randn(*s, dtype=torch.bfloat16) * 0.2)(B, Hkv, Sk, Dh)),
            "wq": _w((lambda *s: torch.randn(*s, dtype=torch.bfloat16) * 0.8)(D, Hq * Dh) * (D ** -0.5)),
            "wo": _w((lambda *s: torch.randn(*s, dtype=torch.bfloat16) * 0.2)(Hq * Dh, D) * ((Hq * Dh) ** -0.5)),
        }
    _A_in = _A_setup()

    def _B_setup():
        B, S, D, F = 1, 1, 640, 1536
        return {
            "x": _w((lambda *s: torch.randn(*s, dtype=torch.bfloat16) * 0.05)(B, S, D)),
            "w1": _w((lambda *s: torch.randn(*s, dtype=torch.bfloat16) * 0.1)(D, F)),
            "b1": _w((lambda *s: torch.randn(*s, dtype=torch.bfloat16) * 0.1)(F)),
            "w2": _w((lambda *s: torch.randn(*s, dtype=torch.bfloat16) * 0.1)(F, D)),
            "b2": _w((lambda *s: torch.randn(*s, dtype=torch.bfloat16) * 0.1)(D)),
        }
    _B_in = _B_setup()

    return {
        "A_x": _A_in["x"],
        "A_k_cache": _A_in["k_cache"],
        "A_v_cache": _A_in["v_cache"],
        "A_wq": _A_in["wq"],
        "A_wo": _A_in["wo"],
        "B_x": _B_in["x"],
        "B_w1": _B_in["w1"],
        "B_b1": _B_in["b1"],
        "B_w2": _B_in["w2"],
        "B_b2": _B_in["b2"],
    }

def reference(A_x, A_k_cache, A_v_cache, A_wq, A_wo, B_x, B_w1, B_b1, B_w2, B_b2):
    def _A_ref(x, k_cache, v_cache, wq, wo):
        B, Sq, D = x.shape
        Hq, Dh = 32, 192
        Hkv, Sk = k_cache.shape[1], k_cache.shape[2]
        W = min(4096, max(256, Sk // 4))
        rep = Hq // Hkv
        q = (lambda x, *d: x.permute(*d))((x @ wq).reshape(B, Sq, Hq, Dh), 0, 2, 1, 3)
        k = (lambda x, n, axis: torch.repeat_interleave(x, n, dim=axis))(k_cache[:, :, Sk - W:, :], rep, axis=1)
        v = (lambda x, n, axis: torch.repeat_interleave(x, n, dim=axis))(v_cache[:, :, Sk - W:, :], rep, axis=1)
        scores = torch.einsum("bhqd,bhkd->bhqk", q, k) * (Dh ** -0.5)
        p = _softmax(scores, axis=-1)
        o = torch.einsum("bhqk,bhkd->bhqd", p, v)
        return (lambda x, *d: x.permute(*d))(o, 0, 2, 1, 3).reshape(B, Sq, Hq * Dh) @ wo

    def _B_ref(x, w1, b1, w2, b2):
        h = torch.einsum("bsd,df->bsf", x, w1) + b1
        zero = torch.zeros_like(h)
        r = torch.where(h > zero, h, zero)
        h = r * r
        out = torch.einsum("bsf,fd->bsd", h, w2) + b2
        return out

    x = A_x
    xn = _layernorm(x, axis=-1)
    s = _A_ref(xn, A_k_cache, A_v_cache, A_wq, A_wo)
    g = _silu(_B_ref(xn, B_w1, B_b1, B_w2, B_b2))
    return x + s * g
```

```cpp
#include <hip/hip_runtime.h>
#include <hip/hip_bf16.h>
#include <stdint.h>

#define GAS __attribute__((address_space(1)))
typedef unsigned short u16;
typedef unsigned long long u64;
typedef unsigned v4u __attribute__((ext_vector_type(4)));
typedef float f32x4 __attribute__((ext_vector_type(4)));
typedef float f32x2 __attribute__((ext_vector_type(2)));
typedef short bf16x8 __attribute__((ext_vector_type(8)));
typedef __bf16 bf16x2 __attribute__((ext_vector_type(2)));

constexpr int DM = 640, NH = 32, DH = 192, SK = 32768, WIN = 4096, FF = 1536;
constexpr int NWG = 256, NTHR = 512;
constexpr int QN = NH * DH;

constexpr int WS_EPOCH = 0;
constexpr int WS_S1 = 4096;
constexpr int WS_ML = WS_S1 + 240 * 64 * 8;
constexpr int WS_OP = WS_ML + 256 * 16;
constexpr int FIN_STRIDE = 176 * 16;
constexpr int WS_FIN = WS_OP + 256 * 192 * 8;
constexpr int WS_END = WS_FIN + 320 * FIN_STRIDE;
#define STAMP(i) do { } while (0)
#define STAMP_DECL do { } while (0)
#define STAMP_FLUSH() do { } while (0)

constexpr int AUX_SC1 = 16;
constexpr unsigned SPIN_CAP = 1u << 18;

#define SB() __builtin_amdgcn_sched_barrier(0)
#define CFENCE() asm volatile("" ::: "memory")
#define WG_BARRIER() do { asm volatile("s_waitcnt lgkmcnt(0)" ::: "memory"); __builtin_amdgcn_s_barrier(); asm volatile("" ::: "memory"); } while (0)

#define AUX_STREAM 2
typedef __amdgpu_buffer_rsrc_t rsrc_t;
__device__ __forceinline__ rsrc_t mkrs(const void* p, unsigned bytes) { return __builtin_amdgcn_make_buffer_rsrc((void*)p, 0, bytes, 0x00020000); }
__device__ __forceinline__ v4u ldst(rsrc_t rs, unsigned voff, unsigned soff) { return __builtin_amdgcn_raw_buffer_load_b128(rs, voff, soff, AUX_STREAM); }
#define POLL_LD(dst, voff, base, imm) asm volatile("global_load_dwordx4 %0, %1, %2 offset:" #imm " sc1" : "=v"(dst) : "v"(voff), "s"(base) : "memory")
__device__ __forceinline__ float dot2(unsigned k, unsigned q, float acc) {
  return __builtin_amdgcn_fdot2_f32_bf16(__builtin_bit_cast(bf16x2, k), __builtin_bit_cast(bf16x2, q), acc, false);
}
__device__ __forceinline__ unsigned cvtpk(float lo, float hi) {
  bf16x2 v = {(__bf16)lo, (__bf16)hi};
  return __builtin_bit_cast(unsigned, v);
}
__device__ __forceinline__ float asf(unsigned u) { return __builtin_bit_cast(float, u); }
__device__ __forceinline__ unsigned asu(float f) { return __builtin_bit_cast(unsigned, f); }
__device__ __forceinline__ float bfr(float f) { return asf(cvtpk(f, f) << 16); }
__device__ __forceinline__ float bflo(unsigned u) { return asf(u << 16); }
__device__ __forceinline__ float bfhi(unsigned u) { return asf(u & 0xffff0000u); }
template <int CTRL> __device__ __forceinline__ float dpp(float x) {
  return __builtin_bit_cast(float, __builtin_amdgcn_mov_dpp(__builtin_bit_cast(int, x), CTRL, 0xf, 0xf, true));
}
constexpr int XOR1 = 0xB1, XOR2 = 0x4E, HMIR = 0x141, ROR8 = 0x128, ROR4 = 0x124;
__device__ __forceinline__ float sum8(float x) { x += dpp<XOR1>(x); x += dpp<XOR2>(x); x += dpp<HMIR>(x); return x; }
__device__ __forceinline__ float max8(float x) { x = fmaxf(x, dpp<XOR1>(x)); x = fmaxf(x, dpp<XOR2>(x)); x = fmaxf(x, dpp<HMIR>(x)); return x; }
__device__ __forceinline__ float sum_hi3(float x) {
  x += dpp<ROR8>(x);
  auto s = __builtin_amdgcn_permlane16_swap(asu(x), asu(x), false, false);
  x = asf(s[0]) + asf(s[1]);
  auto t = __builtin_amdgcn_permlane32_swap(asu(x), asu(x), false, false);
  return asf(t[0]) + asf(t[1]);
}
__device__ __forceinline__ float max_hi3(float x) {
  x = fmaxf(x, dpp<ROR8>(x));
  auto s = __builtin_amdgcn_permlane16_swap(asu(x), asu(x), false, false);
  x = fmaxf(asf(s[0]), asf(s[1]));
  auto t = __builtin_amdgcn_permlane32_swap(asu(x), asu(x), false, false);
  return fmaxf(asf(t[0]), asf(t[1]));
}
__device__ __forceinline__ f32x4 mfma(v4u a, v4u b, f32x4 c) {
  return __builtin_amdgcn_mfma_f32_16x16x32_bf16(__builtin_bit_cast(bf16x8, a), __builtin_bit_cast(bf16x8, b), c, 0, 0, 0);
}
__device__ __forceinline__ v4u sel(unsigned pp, const v4u& m) { return v4u{pp & m[0], pp & m[1], pp & m[2], pp & m[3]}; }
__device__ __forceinline__ f32x2 combine_halves(f32x4 d) {
  float z[4];
#pragma unroll
  for (int i = 0; i < 4; ++i)
    z[i] = asf((unsigned)__builtin_amdgcn_update_dpp((int)asu(d[i]), (int)asu(d[i]), ROR8, 0xC, 0xf, false));
  auto a = __builtin_amdgcn_permlane32_swap(asu(z[0]), asu(z[2]), false, false);
  auto c = __builtin_amdgcn_permlane32_swap(asu(z[1]), asu(z[3]), false, false);
  return f32x2{asf(a[0]) + asf(a[1]), asf(c[0]) + asf(c[1])};
}
__device__ __forceinline__ v4u gran2(float v0, float v1, unsigned tagA, unsigned tagB) {
  return v4u{(asu(v0) & 0xffffff00u) | tagB, tagA, (asu(v1) & 0xffffff00u) | tagB, tagA};
}
__device__ __forceinline__ unsigned gbad(const v4u& g, unsigned tagA, unsigned tagB) {
  return (g[1] ^ tagA) | (g[3] ^ tagA) | (((g[0] ^ tagB) | (g[2] ^ tagB)) & 0xffu);
}
__device__ __forceinline__ bool any_bad(unsigned bad) { return __builtin_amdgcn_ballot_w64(bad != 0u) != 0ull; }
__device__ __forceinline__ float gval(unsigned u) { return asf(u & 0xffffff00u); }

struct alignas(16) Lds {
  float red[8][2][192];
  unsigned qh[128];
  float wm[8];
  float wl[8];
};

__global__ void __launch_bounds__(NTHR, 2)
fused_decode_kernel(const u16* __restrict__ x_, const u16* __restrict__ kc_, const u16* __restrict__ vc_, const u16* __restrict__ wq_,
                    const u16* __restrict__ wo_, const u16* __restrict__ w1_, const u16* __restrict__ b1_, const u16* __restrict__ w2_,
                    const u16* __restrict__ b2_, u16* __restrict__ out_, unsigned char* __restrict__ ws_) {
  __shared__ Lds L;
  STAMP_DECL;
  STAMP(0);
  asm volatile("" :: "s"(x_), "s"(kc_), "s"(vc_), "s"(wq_), "s"(wo_), "s"(w1_), "s"(b1_), "s"(w2_), "s"(b2_), "s"(out_), "s"(ws_));
  const int tid = threadIdx.x, lane = tid & 63, w = __builtin_amdgcn_readfirstlane(tid >> 6), r = lane >> 3, c = lane & 7;
  const int b = blockIdx.x, h = (b & 7) * 4 + ((b >> 3) & 3), j = b >> 5;
  GAS unsigned char* ws = (GAS unsigned char*)ws_;
  const GAS unsigned char* x = (const GAS unsigned char*)x_;
  const rsrc_t rs = mkrs(ws_, WS_END);

  const bool isq = j < 6;
  const int u1 = (j == 6) ? h : 32 + h;
  const bool has1 = isq || u1 < 48;
  const int cg = isq ? 3 * h + (j >> 1) : 96 + (u1 >> 1), half = (isq ? j : u1) & 1;
  const unsigned stride = isq ? QN * 2 : FF * 2;
  const rsrc_t rw1 = mkrs((const unsigned char*)(isq ? wq_ : w1_) + (isq ? cg : cg - 96) * 128, has1 ? DM * stride : 0u);
  const int row0 = half * 320 + w * 8 + r;
  v4u W1[5]; unsigned xr[5];
  {
    const unsigned off = row0 * stride + c * 16;
#pragma unroll
    for (int p = 0; p < 5; ++p) W1[p] = ldst(rw1, off, p * 64 * stride);
  }
  const v4u xa = *(const GAS v4u*)(x + lane * 16);
  const v4u xb = *(const GAS v4u*)(x + 1024 + (lane & 15) * 16);
#pragma unroll
  for (int p = 0; p < 5; ++p) xr[p] = *(const GAS u16*)(x + (row0 + p * 64) * 2);
  const int vu = b < 240 ? b : 239, rg = vu / 10, cgrp = vu - rg * 10;
  unsigned b1p = *(const GAS unsigned*)((const GAS unsigned char*)b1_ + rg * 128 + (lane & 31) * 4);
  const int cp = (w == 0) ? b : 256 + (b & 63);
  unsigned xp = *(const GAS unsigned*)(x + cp * 4), b2p = *(const GAS unsigned*)((const GAS unsigned char*)b2_ + cp * 4);
  const u64 ep = __hip_atomic_load((GAS u64*)(ws + WS_EPOCH), __ATOMIC_RELAXED, __HIP_MEMORY_SCOPE_AGENT);
  const size_t kvoff = ((size_t)h * SK + (SK - WIN) + j * 512 + w * 64) * (DH * 2);
  const rsrc_t rk = mkrs((const unsigned char*)kc_ + kvoff, 64 * DH * 2), rv = mkrs((const unsigned char*)vc_ + kvoff, 64 * DH * 2);
  const unsigned loff = (unsigned)(r * (DH * 2) + c * 16);
  v4u K[8][3], V[8][3];
#pragma unroll
  for (int bk = 0; bk < 8; ++bk)
#pragma unroll
    for (int i = 0; i < 3; ++i) K[bk][i] = ldst(rk, loff + i * 128, bk * 3072);
  SB(); CFENCE();
  STAMP(1);

  v4u msk;
#pragma unroll
  for (int d = 0; d < 4; ++d) msk[d] = ((c >> 1) == d) ? ((c & 1) ? 0xffff0000u : 0x0000ffffu) : 0u;

  float mean_b, rden;
  {
    const bool hb = lane < 16;
    float e[16];
#pragma unroll
    for (int k = 0; k < 4; ++k) { e[2 * k] = bflo(xa[k]); e[2 * k + 1] = bfhi(xa[k]); e[8 + 2 * k] = bflo(xb[k]); e[9 + 2 * k] = bfhi(xb[k]); }
    float s = 0.f, s2 = 0.f;
#pragma unroll
    for (int k = 0; k < 8; ++k) { s += e[k]; s2 += e[8 + k]; }
    s += hb ? s2 : 0.f;
    s = sum_hi3(sum8(s));
    const float mean = s * (1.f / DM);
    float v = 0.f, v2 = 0.f;
#pragma unroll
    for (int k = 0; k < 8; ++k) { const float d0 = e[k] - mean, d1 = e[8 + k] - mean; v = fmaf(d0, d0, v); v2 = fmaf(d1, d1, v2); }
    v += hb ? v2 : 0.f;
    v = sum_hi3(sum8(v));
    mean_b = bfr(mean);
    rden = __builtin_amdgcn_rcpf(bfr(__builtin_amdgcn_sqrtf(bfr(bfr(v * (1.f / DM)) + 1e-5f))));
  }
  asm volatile("" : "+v"(b1p), "+v"(xp), "+v"(b2p));
  const u64 tg = ep + 1;
  const unsigned tagA = __builtin_amdgcn_readfirstlane((unsigned)tg), tagH = __builtin_amdgcn_readfirstlane((unsigned)(tg >> 32)), tagB = tagH & 0xffu;
  const bool dvalid = ((lane >> 3) & 1) == (lane >> 5);
  const int dpos = c * 8 + ((lane >> 4) & 1) * 4;
  {
    f32x4 acc = {0.f, 0.f, 0.f, 0.f};
#pragma unroll
    for (int p = 0; p < 5; ++p) {
      const float xn = bfr(bflo(xr[p]) - mean_b) * rden;
      acc = mfma(sel(cvtpk(xn, xn), msk), W1[p], acc);
    }
    if (dvalid) *(f32x4*)&L.red[w][lane >> 5][dpos] = acc;
  }
  WG_BARRIER();
  if (w == 7 && has1 && lane < 32) {
    f32x2 s = {0.f, 0.f};
#pragma unroll
    for (int ww = 0; ww < 8; ++ww) { s += *(const f32x2*)&L.red[ww][0][2 * lane]; s += *(const f32x2*)&L.red[ww][1][2 * lane]; }
    __builtin_amdgcn_raw_buffer_store_b128(gran2(s[0], s[1], tagA, tagB), rs, WS_S1 + (cg * 2 + half) * 512 + lane * 16, 0, AUX_SC1);
  }
  STAMP(2);
#pragma unroll
  for (int bk = 0; bk < 4; ++bk)
#pragma unroll
    for (int i = 0; i < 3; ++i) asm volatile("" : "+v"(K[bk][i]));
  STAMP(3);
  v4u g0, g1;
  const unsigned goff = WS_S1 + (tid < 96 ? (3 * h + (tid >> 5)) * 1024 + (tid & 31) * 16 : (96 + rg) * 1024 + (tid & 31) * 16);
  if (w < 2) { POLL_LD(g0, goff, ws, 0); POLL_LD(g1, goff, ws, 512); }
#pragma unroll
  for (int bk = 0; bk < 8; ++bk)
#pragma unroll
    for (int i = 0; i < 3; ++i) V[bk][i] = ldst(rv, loff + i * 128, bk * 3072);
  SB(); CFENCE();
  STAMP(4);
  if (w < 2) {
    asm volatile("s_waitcnt vmcnt(24)" : "+v"(g0), "+v"(g1) :: "memory");
    unsigned spins = 0;
    while (any_bad(gbad(g0, tagA, tagB) | gbad(g1, tagA, tagB))) {
      if (++spins > SPIN_CAP) break;
      __builtin_amdgcn_s_sleep(1);
      asm volatile("global_load_dwordx4 %0, %2, %3 sc1\n\tglobal_load_dwordx4 %1, %2, %3 offset:512 sc1\n\ts_waitcnt vmcnt(0)"
                   : "=&v"(g0), "=&v"(g1) : "v"(goff), "s"(ws) : "memory");
    }
    float a0 = gval(g0[0]) + gval(g1[0]), a1 = gval(g0[2]) + gval(g1[2]);
    if (tid >= 96) {
      a0 = fmaxf(bfr(bfr(a0) + bflo(b1p)), 0.f); a1 = fmaxf(bfr(bfr(a1) + bfhi(b1p)), 0.f);
      a0 *= a0; a1 *= a1;
    }
    L.qh[tid] = cvtpk(a0, a1);
  }
  WG_BARRIER();
  STAMP(5);
  float sc[8];
  {
    v4u q[3];
#pragma unroll
    for (int i = 0; i < 3; ++i) q[i] = *(const v4u*)&L.qh[i * 32 + c * 4];
    constexpr float C2 = 0.07216878364870322f * 1.4426950408889634f;
#pragma unroll
    for (int bk = 0; bk < 8; ++bk) {
      float d = 0.f;
#pragma unroll
      for (int i = 0; i < 3; ++i)
#pragma unroll
        for (int k = 0; k < 4; ++k) d = dot2(K[bk][i][k], q[i][k], d);
      sc[bk] = sum8(d) * C2;
    }
  }
  SB();
  const int q4 = j >> 1, chalf = j & 1;
  const bool fin = (w == 0) || (w == 1 && b < 64);
  v4u WL[6];
  {
    const bool iso = w < 5;
    const unsigned char* base = iso ? (const unsigned char*)wo_ + (size_t)(h * DH + 48 * q4) * (DM * 2) + chalf * 640 + w * 128
                                    : (const unsigned char*)w2_ + (size_t)(rg * 64 + (w - 5) * 32) * (DM * 2) + cgrp * 128;
    const rsrc_t rl = mkrs(base, iso ? 48 * DM * 2 : ((w < 7 && b < 240) ? 32 * DM * 2 : 0));
    const unsigned off = (iso ? 2 * r : r) * (DM * 2) + c * 16;
#pragma unroll
    for (int i = 0; i < 6; ++i) WL[i] = ldst(rl, off, (iso ? (i >> 1) * 16 + (i & 1) : i * 8) * (DM * 2));
  }
  SB(); CFENCE();
  {
    float m = sc[0];
#pragma unroll
    for (int bk = 1; bk < 8; ++bk) m = fmaxf(m, sc[bk]);
    m = max_hi3(m);
    if (lane == 0) L.wm[w] = m;
  }
  WG_BARRIER();
  float M;
  {
    const f32x4 m0 = *(const f32x4*)&L.wm[0], m1 = *(const f32x4*)&L.wm[4];
    M = fmaxf(fmaxf(fmaxf(m0[0], m0[1]), fmaxf(m0[2], m0[3])), fmaxf(fmaxf(m1[0], m1[1]), fmaxf(m1[2], m1[3])));
  }
  v4u A[8];
  {
    float l = 0.f;
#pragma unroll
    for (int bk = 0; bk < 8; ++bk) {
      const float p = __builtin_amdgcn_exp2f(sc[bk] - M);
      const unsigned pp = cvtpk(p, p);
      l += bfhi(pp);
      A[bk] = sel(pp, msk);
    }
    l = sum_hi3(l);
    if (lane == 0) L.wl[w] = l;
  }
  SB();
  STAMP(6);
  {
    f32x4 acc[3] = {{0.f, 0.f, 0.f, 0.f}, {0.f, 0.f, 0.f, 0.f}, {0.f, 0.f, 0.f, 0.f}};
#pragma unroll
    for (int bk = 0; bk < 8; ++bk) {
#pragma unroll
      for (int i = 0; i < 3; ++i) acc[i] = mfma(A[bk], V[bk][i], acc[i]);
      SB();
    }
    if (dvalid) {
#pragma unroll
      for (int i = 0; i < 3; ++i) *(f32x4*)&L.red[w][lane >> 5][i * 64 + dpos] = acc[i];
    }
  }
  STAMP(7);
  WG_BARRIER();
  const int fslot = (lane >> 5) + 2 * ((lane >> 4) & 1) + 4 * c;
  if (w >= 5) {
    if (w >= 6) {
      const int t = tid - 384;
      if (t < 96) {
        f32x2 s = {0.f, 0.f};
#pragma unroll
        for (int ww = 0; ww < 8; ++ww) { s += *(const f32x2*)&L.red[ww][0][2 * t]; s += *(const f32x2*)&L.red[ww][1][2 * t]; }
        __builtin_amdgcn_raw_buffer_store_b128(gran2(s[0], s[1], tagA, tagB), rs, WS_OP + (h * 8 + j) * 1536 + t * 16, 0, AUX_SC1);
      } else if (t == 96) {
        const f32x4 l0 = *(const f32x4*)&L.wl[0], l1 = *(const f32x4*)&L.wl[4];
        const float l = ((l0[0] + l0[1]) + (l0[2] + l0[3])) + ((l1[0] + l1[1]) + (l1[2] + l1[3]));
        __builtin_amdgcn_raw_buffer_store_b128(v4u{asu(M), tagA, (asu(l) & 0xffffff00u) | tagB, tagA}, rs, WS_ML + (h * 8 + j) * 16, 0, AUX_SC1);
      }
    }
    STAMP(8);
    if (w < 7 && b < 240) {
      f32x4 acc = {0.f, 0.f, 0.f, 0.f};
      const u16* h16 = (const u16*)&L.qh[96] + (w - 5) * 32;
#pragma unroll
      for (int i = 0; i < 4; ++i) { const unsigned hv = h16[i * 8 + r]; acc = mfma(sel(hv | (hv << 16), msk), WL[i], acc); }
      const f32x2 o = combine_halves(acc);
      if (!(lane & 8)) __builtin_amdgcn_raw_buffer_store_b128(gran2(o[0], o[1], tagA, tagB), rs, WS_FIN + (cgrp * 32 + fslot) * FIN_STRIDE + (128 + rg * 2 + (w - 5)) * 16, 0, AUX_SC1);
    }
    asm volatile("" :: "v"(WL[0]), "v"(WL[1]), "v"(WL[2]), "v"(WL[3]), "v"(WL[4]), "v"(WL[5]));
    STAMP(9);
    STAMP_FLUSH();
    return;
  }
  {
    const unsigned ooff = WS_OP + (h * 8 + c) * 1536 + (48 * q4 + 2 * r) * 8;
    const unsigned moff = WS_ML + (h * 8 + c) * 16;
    v4u go[3], gm;
    unsigned spins = 0;
    for (;;) {
      asm volatile("global_load_dwordx4 %0, %4, %6 sc1\n\tglobal_load_dwordx4 %1, %4, %6 offset:128 sc1\n\tglobal_load_dwordx4 %2, %4, %6 offset:256 sc1\n\t"
                   "global_load_dwordx4 %3, %5, %6 sc1\n\ts_waitcnt vmcnt(0)"
                   : "=&v"(go[0]), "=&v"(go[1]), "=&v"(go[2]), "=&v"(gm) : "v"(ooff), "v"(moff), "s"(ws) : "memory");
      const unsigned bad = gbad(go[0], tagA, tagB) | gbad(go[1], tagA, tagB) | gbad(go[2], tagA, tagB) |
                           (gm[1] ^ tagA) | (gm[3] ^ tagA) | ((gm[2] ^ tagB) & 0xffu);
      if (!any_bad(bad) || ++spins > SPIN_CAP) break;
      __builtin_amdgcn_s_sleep(1);
    }
    STAMP(8);
    const float mc = asf(gm[0]), Mh = max8(mc);
    const float e = __builtin_amdgcn_exp2f(mc - Mh);
    const float f = e * __builtin_amdgcn_rcpf(sum8(e * gval(gm[2])));
    f32x4 acc = {0.f, 0.f, 0.f, 0.f};
#pragma unroll
    for (int i = 0; i < 6; ++i) {
      const float o = sum8(f * gval(go[i >> 1][(i & 1) * 2]));
      acc = mfma(sel(cvtpk(o, o), msk), WL[i], acc);
    }
    const f32x2 o = combine_halves(acc);
    if (!(lane & 8)) __builtin_amdgcn_raw_buffer_store_b128(gran2(o[0], o[1], tagA, tagB), rs, WS_FIN + (chalf * 160 + w * 32 + fslot) * FIN_STRIDE + (h * 4 + q4) * 16, 0, AUX_SC1);
  }
  STAMP(9);
  if (fin) {
    const unsigned foff = WS_FIN + cp * FIN_STRIDE + lane * 16;
    const unsigned f2 = WS_FIN + cp * FIN_STRIDE + 2048 + (lane < 48 ? lane : 47) * 16;
    v4u a0, a1, a2;
    unsigned spins = 0;
    for (;;) {
      asm volatile("global_load_dwordx4 %0, %3, %5 sc1\n\tglobal_load_dwordx4 %1, %3, %5 offset:1024 sc1\n\tglobal_load_dwordx4 %2, %4, %5 sc1\n\ts_waitcnt vmcnt(0)"
                   : "=&v"(a0), "=&v"(a1), "=&v"(a2) : "v"(foff), "v"(f2), "s"(ws) : "memory");
      const unsigned bad = gbad(a0, tagA, tagB) | gbad(a1, tagA, tagB) | gbad(a2, tagA, tagB);
      if (!any_bad(bad) || ++spins > SPIN_CAP) break;
      __builtin_amdgcn_s_sleep(1);
    }
    STAMP(10);
    const float s0 = gval(a0[0]) + gval(a1[0]), s1 = gval(a0[2]) + gval(a1[2]);
    const float m0 = lane < 48 ? gval(a2[0]) : 0.f, m1 = lane < 48 ? gval(a2[2]) : 0.f;
    auto u = __builtin_amdgcn_permlane32_swap(asu(s0), asu(s1), false, false);
    auto v = __builtin_amdgcn_permlane32_swap(asu(m0), asu(m1), false, false);
    const float su = asf(u[0]) + asf(u[1]), sv = asf(v[0]) + asf(v[1]);
    auto t = __builtin_amdgcn_permlane16_swap(asu(su), asu(sv), false, false);
    float z = asf(t[0]) + asf(t[1]);
    z += dpp<ROR8>(z); z = sum8(z);
    const float S0 = asf(__builtin_amdgcn_readlane(asu(z), 0)), O0 = asf(__builtin_amdgcn_readlane(asu(z), 16));
    const float S1 = asf(__builtin_amdgcn_readlane(asu(z), 32)), O1 = asf(__builtin_amdgcn_readlane(asu(z), 48));
    const float t0 = bfr(bfr(O0) + bflo(b2p)), t1 = bfr(bfr(O1) + bfhi(b2p));
    constexpr float L2E = 1.4426950408889634f;
    const float ga = bfr(t0 * __builtin_amdgcn_rcpf(1.f + __builtin_amdgcn_exp2f(-L2E * t0)));
    const float gb = bfr(t1 * __builtin_amdgcn_rcpf(1.f + __builtin_amdgcn_exp2f(-L2E * t1)));
    const float y0 = bflo(xp) + bfr(bfr(S0) * ga), y1 = bfhi(xp) + bfr(bfr(S1) * gb);
    if (lane == 0) {
      *(GAS unsigned*)((GAS unsigned char*)out_ + cp * 4) = cvtpk(y0, y1);
      if (cp == 0) __hip_atomic_store((GAS u64*)(ws + WS_EPOCH), ((u64)tagH << 32) | tagA, __ATOMIC_RELAXED, __HIP_MEMORY_SCOPE_AGENT);
    }
    STAMP(11);
  }
  STAMP_FLUSH();
}

extern "C" void kernel_launch(void* const* d_in, const int* in_sizes, int n_in, void* d_out, int out_size, void* d_ws, size_t ws_size,
                              hipStream_t stream) {
  (void)in_sizes; (void)n_in; (void)out_size; (void)ws_size;
  fused_decode_kernel<<<NWG, NTHR, 0, stream>>>((const u16*)d_in[0], (const u16*)d_in[1], (const u16*)d_in[2], (const u16*)d_in[3],
                                                (const u16*)d_in[4], (const u16*)d_in[6], (const u16*)d_in[7], (const u16*)d_in[8],
                                                (const u16*)d_in[9], (u16*)d_out, (unsigned char*)d_ws);
}
```

```cpp
#define DEPTH 3
#define WO_LAST
#include <hip/hip_runtime.h>
#include <hip/hip_bf16.h>
#include <stdint.h>

#define GAS __attribute__((address_space(1)))
typedef unsigned short u16;
typedef unsigned long long u64;
typedef unsigned v4u __attribute__((ext_vector_type(4)));
typedef float f32x4 __attribute__((ext_vector_type(4)));
typedef float f32x2 __attribute__((ext_vector_type(2)));
typedef short bf16x8 __attribute__((ext_vector_type(8)));
typedef __bf16 bf16x2 __attribute__((ext_vector_type(2)));

constexpr int DM = 640, NH = 32, DH = 192, SK = 32768, WIN = 4096, FF = 1536;
constexpr int NWG = 256, NTHR = 512;
constexpr int QN = NH * DH;

constexpr int WS_EPOCH = 0;
constexpr int WS_S1 = 4096;
constexpr int WS_ML = WS_S1 + 240 * 64 * 8;
constexpr int WS_OP = WS_ML + 256 * 128;
constexpr int FIN_STRIDE = 72 * 128;
constexpr int WS_FIN = WS_OP + 256 * 192 * 8;
constexpr int WS_END = WS_FIN + 40 * FIN_STRIDE;
#define STAMP(i) do { } while (0)
#define STAMP0() do { } while (0)
#define STAMP_DECL do { } while (0)
#define SETX(i, v) do { } while (0)
#define STAMP_FLUSH() do { } while (0)

constexpr int AUX_SC1 = 16;
constexpr unsigned SPIN_CAP = 1u << 18;

#define SB() __builtin_amdgcn_sched_barrier(0)
#define CFENCE() asm volatile("" ::: "memory")
#define WG_BARRIER() do { asm volatile("s_waitcnt lgkmcnt(0)" ::: "memory"); __builtin_amdgcn_s_barrier(); asm volatile("" ::: "memory"); } while (0)

#define AUX_STREAM 2
typedef __amdgpu_buffer_rsrc_t rsrc_t;
__device__ __forceinline__ rsrc_t mkrs(const void* p, unsigned bytes) { return __builtin_amdgcn_make_buffer_rsrc((void*)p, 0, bytes, 0x00020000); }
__device__ __forceinline__ v4u ldst(rsrc_t rs, unsigned voff, unsigned soff) { return __builtin_amdgcn_raw_buffer_load_b128(rs, voff, soff, AUX_STREAM); }
#define POLL_LD(dst, voff, base, imm) asm volatile("global_load_dwordx4 %0, %1, %2 offset:" #imm " sc1" : "=v"(dst) : "v"(voff), "s"(base) : "memory")
__device__ __forceinline__ float dot2(unsigned k, unsigned q, float acc) {
  return __builtin_amdgcn_fdot2_f32_bf16(__builtin_bit_cast(bf16x2, k), __builtin_bit_cast(bf16x2, q), acc, false);
}
__device__ __forceinline__ unsigned cvtpk(float lo, float hi) {
  bf16x2 v = {(__bf16)lo, (__bf16)hi};
  return __builtin_bit_cast(unsigned, v);
}
__device__ __forceinline__ float asf(unsigned u) { return __builtin_bit_cast(float, u); }
__device__ __forceinline__ unsigned asu(float f) { return __builtin_bit_cast(unsigned, f); }
__device__ __forceinline__ float bfr(float f) { return asf(cvtpk(f, f) << 16); }
__device__ __forceinline__ float bflo(unsigned u) { return asf(u << 16); }
__device__ __forceinline__ float bfhi(unsigned u) { return asf(u & 0xffff0000u); }
template <int CTRL> __device__ __forceinline__ float dpp(float x) {
  return __builtin_bit_cast(float, __builtin_amdgcn_mov_dpp(__builtin_bit_cast(int, x), CTRL, 0xf, 0xf, true));
}
constexpr int XOR1 = 0xB1, XOR2 = 0x4E, HMIR = 0x141, ROR8 = 0x128, ROR4 = 0x124;
__device__ __forceinline__ float sum8(float x) { x += dpp<XOR1>(x); x += dpp<XOR2>(x); x += dpp<HMIR>(x); return x; }
__device__ __forceinline__ float max8(float x) { x = fmaxf(x, dpp<XOR1>(x)); x = fmaxf(x, dpp<XOR2>(x)); x = fmaxf(x, dpp<HMIR>(x)); return x; }
__device__ __forceinline__ float sum_hi3(float x) {
  x += dpp<ROR8>(x);
  auto s = __builtin_amdgcn_permlane16_swap(asu(x), asu(x), false, false);
  x = asf(s[0]) + asf(s[1]);
  auto t = __builtin_amdgcn_permlane32_swap(asu(x), asu(x), false, false);
  return asf(t[0]) + asf(t[1]);
}
__device__ __forceinline__ float max_hi3(float x) {
  x = fmaxf(x, dpp<ROR8>(x));
  auto s = __builtin_amdgcn_permlane16_swap(asu(x), asu(x), false, false);
  x = fmaxf(asf(s[0]), asf(s[1]));
  auto t = __builtin_amdgcn_permlane32_swap(asu(x), asu(x), false, false);
  return fmaxf(asf(t[0]), asf(t[1]));
}
__device__ __forceinline__ f32x4 mfma(v4u a, v4u b, f32x4 c) {
  return __builtin_amdgcn_mfma_f32_16x16x32_bf16(__builtin_bit_cast(bf16x8, a), __builtin_bit_cast(bf16x8, b), c, 0, 0, 0);
}
__device__ __forceinline__ v4u sel(unsigned pp, const v4u& m) { return v4u{pp & m[0], pp & m[1], pp & m[2], pp & m[3]}; }
template <int ROW> __device__ __forceinline__ f32x2 xw_sum(const float* red, int pair, int lane) {
  const float* p = red + (lane >> 4) * (4 * ROW) + 2 * pair;
  const f32x2 s = (*(const f32x2*)p + *(const f32x2*)(p + ROW)) + (*(const f32x2*)(p + 2 * ROW) + *(const f32x2*)(p + 3 * ROW));
  auto a = __builtin_amdgcn_permlane16_swap(asu(s[0]), asu(s[0]), false, false);
  auto c = __builtin_amdgcn_permlane16_swap(asu(s[1]), asu(s[1]), false, false);
  const float u = asf(a[0]) + asf(a[1]), v = asf(c[0]) + asf(c[1]);
  auto d = __builtin_amdgcn_permlane32_swap(asu(u), asu(u), false, false);
  auto e = __builtin_amdgcn_permlane32_swap(asu(v), asu(v), false, false);
  return f32x2{asf(d[0]) + asf(d[1]), asf(e[0]) + asf(e[1])};
}
__device__ __forceinline__ v4u gran2(float v0, float v1, unsigned tagA, unsigned tagB) {
  return v4u{(asu(v0) & 0xffffff00u) | tagB, tagA, (asu(v1) & 0xffffff00u) | tagB, tagA};
}
__device__ __forceinline__ unsigned gbad(const v4u& g, unsigned tagA, unsigned tagB) {
  return (g[1] ^ tagA) | (g[3] ^ tagA) | (((g[0] ^ tagB) | (g[2] ^ tagB)) & 0xffu);
}
__device__ __forceinline__ bool any_bad(unsigned bad) { return __builtin_amdgcn_ballot_w64(bad != 0u) != 0ull; }
__device__ __forceinline__ float gval(unsigned u) { return asf(u & 0xffffff00u); }

constexpr int RA = 200, RB = 136, RS = 72;
struct alignas(16) Lds {
  float redA[16 * RA];
  float redB[16 * RB];
  float red1[16 * RS];
  float red2[16 * RS];
  unsigned qh[128];
  float wm[8];
  float wl[8];
  u16 xs[640];
};

__global__ void __launch_bounds__(NTHR, 2)
fused_decode_kernel(const u16*, const u16*, const u16*, const u16*, const u16*, const u16*, const u16*, const u16*, const u16*, u16*, unsigned char*) {
  typedef unsigned v16u __attribute__((ext_vector_type(16)));
  v16u ka; v4u kb; u64 kw;
  int b = blockIdx.x;
  asm volatile("s_load_dwordx16 %0, %4, 0x0\n\ts_load_dwordx4 %1, %4, 0x40\n\ts_load_dwordx2 %2, %4, 0x50"
               : "=&s"(ka), "=&s"(kb), "=&s"(kw), "+s"(b) : "s"(__builtin_amdgcn_kernarg_segment_ptr()));
  SB();
  __shared__ Lds L;
  STAMP_DECL;
  STAMP0();
  asm volatile("" ::: "v135");
  const int tid = threadIdx.x, lane = tid & 63, w = __builtin_amdgcn_readfirstlane(tid >> 6), r = lane >> 3, c = lane & 7;
  const int h = (b & 7) * 4 + ((b >> 3) & 3), j = b >> 5;
  const bool isq = j < 6;
  const int u1 = (j == 6) ? h : 32 + h;
  const bool has1 = isq || u1 < 48;
  const int mq = (j - 6) >> 31;
  int cg = ((3 * h + (j >> 1)) & mq) | ((96 + (u1 >> 1)) & ~mq), half = ((j & mq) | (u1 & ~mq)) & 1;
  unsigned stride = FF * 2 + ((QN * 2 - FF * 2) & mq);
  unsigned o1 = (cg - (96 & ~mq)) * 128, n1 = has1 ? DM * stride : 0u;
  unsigned off1 = (half * 320 + w * 8 + r) * stride + c * 16;
  unsigned kvrow = h * SK + (SK - WIN) + j * 512 + w * 8;
  u64 kvoff = (u64)kvrow * (DH * 2);
  unsigned loff = (unsigned)(r * (DH * 2) + c * 16);
  const int vu = b < 240 ? b : 239;
  int rg = vu / 10, cgrp = vu - rg * 10;
  unsigned o2 = (rg * 64 + w * 8) * (DM * 2) + cgrp * 128;
  const int P = h >> 1, jj = (h & 1) * 8 + j;
  int rgp = jj / 5, cg5 = jj - rgp * 5;
  const bool isred = jj == 15;
  const int rowb = 128 * rgp + 16 * w;
  unsigned ol = (P * 384 + rowb) * (DM * 2) + cg5 * 256;
  unsigned offl = (lane >> 4) * (4 * DM * 2) + (lane & 15) * 16, off2 = r * (DM * 2) + c * 16, offx = lane * 16, offx2 = (lane & 15) * 16;
  asm volatile("" : "+s"(cg), "+s"(half), "+s"(stride), "+s"(o1), "+s"(n1), "+s"(kvoff), "+s"(rg), "+s"(cgrp), "+s"(o2), "+s"(rgp), "+s"(cg5), "+s"(ol));
  asm volatile("" : "+v"(off1), "+v"(loff), "+v"(offl), "+v"(off2), "+v"(offx), "+v"(offx2));
  SB();
  asm volatile("s_waitcnt lgkmcnt(0)" : "+s"(ka), "+s"(kb), "+s"(kw));
#define KARG(v, i) ((const unsigned char*)(((u64)(v)[2 * (i) + 1] << 32) | (v)[2 * (i)]))
  const unsigned char *x_ = KARG(ka, 0), *kc_ = KARG(ka, 1), *vc_ = KARG(ka, 2), *wq_ = KARG(ka, 3), *wo_ = KARG(ka, 4), *w1_ = KARG(ka, 5), *b1_ = KARG(ka, 6),
                      *w2_ = KARG(ka, 7), *b2_ = KARG(kb, 0), *out_ = KARG(kb, 1), *ws_ = (const unsigned char*)kw;
  GAS unsigned char* ws = (GAS unsigned char*)ws_;
  const GAS unsigned char* x = (const GAS unsigned char*)x_;
  const rsrc_t rw1 = mkrs((const unsigned char*)(isq ? wq_ : w1_) + o1, n1);
  rsrc_t RW2, RWO, RK, RV;
  constexpr int I_W2 = 5, I_K = 6, I_V = 30, I_L = 54, I_END = 58;
  v4u R[I_END];
#define ISSUE(i) do { if constexpr ((i) < I_W2) R[i] = ldst(rw1, off1, (i) * 64 * stride); \
    else if constexpr ((i) == I_W2) R[i] = ldst(RW2, off2, 0); \
    else if constexpr ((i) >= I_K && (i) < I_K + 24) R[i] = ldst(RK, loff + (((i) - I_K) % 3) * 128, (((i) - I_K) / 3) * 24576); \
    else if constexpr ((i) >= I_V && (i) < I_V + 24) R[i] = ldst(RV, loff + (((i) - I_V) % 3) * 128, (((i) - I_V) / 3) * 24576); \
    else if constexpr ((i) >= I_L && (i) < I_L + 4) R[i] = ldst(RWO, offl + ((i) - I_L) * (DM * 2), 0); \
    SB(); } while (0)
#define TOUCH(i) do { asm volatile("" : "+v"(R[i])); SB(); } while (0)
#define STEP(i) do { TOUCH(i); ISSUE((i) + DEPTH); } while (0)
#define STEP3(i) do { STEP(i); STEP((i) + 1); STEP((i) + 2); } while (0)

  const v4u xa = *(const GAS v4u*)(x + offx);
  const v4u xb = *(const GAS v4u*)(x + 1024 + offx2);
  SB();
  ISSUE(0); ISSUE(1); ISSUE(2);
  if constexpr (DEPTH > 3) ISSUE(3);
  static_assert(DEPTH == 3 || DEPTH == 4, "issue schedule");
  u64 ep;
  asm volatile("s_load_dwordx2 %0, %1, 0x0" : "=s"(ep) : "s"(ws_) : "memory");
  SB();
  STAMP(1);
  SETX(15, (unsigned)__builtin_amdgcn_s_getreg(20 | (3 << 11)) & 0xfu);
  const rsrc_t rs = mkrs(ws_, WS_END);
  RW2 = mkrs((const unsigned char*)w2_ + o2, b < 240 ? 8 * DM * 2 : 0);
  RWO = mkrs((const unsigned char*)wo_ + ol, isred ? 0 : 16 * DM * 2);
  RK = mkrs((const unsigned char*)kc_ + kvoff, 512 * DH * 2);
  RV = mkrs((const unsigned char*)vc_ + kvoff, 512 * DH * 2);

  v4u msk;
#pragma unroll
  for (int d = 0; d < 4; ++d) msk[d] = ((c >> 1) == d) ? ((c & 1) ? 0xffff0000u : 0x0000ffffu) : 0u;

  float mean_b, rden;
  unsigned xr[5];
  {
    const bool hb = lane < 16;
    *(v4u*)&L.xs[lane * 8] = xa;
    if (hb) *(v4u*)&L.xs[512 + lane * 8] = xb;
#pragma unroll
    for (int p = 0; p < 5; ++p) xr[p] = L.xs[half * 320 + p * 64 + w * 8 + r];
    float e[16];
#pragma unroll
    for (int k = 0; k < 4; ++k) { e[2 * k] = bflo(xa[k]); e[2 * k + 1] = bfhi(xa[k]); e[8 + 2 * k] = bflo(xb[k]); e[9 + 2 * k] = bfhi(xb[k]); }
    float s = 0.f, s2 = 0.f;
#pragma unroll
    for (int k = 0; k < 8; ++k) { s += e[k]; s2 += e[8 + k]; }
    s += hb ? s2 : 0.f;
    s = sum_hi3(sum8(s));
    const float mean = s * (1.f / DM);
    float v = 0.f, v2 = 0.f;
#pragma unroll
    for (int k = 0; k < 8; ++k) { const float d0 = e[k] - mean, d1 = e[8 + k] - mean; v = fmaf(d0, d0, v); v2 = fmaf(d1, d1, v2); }
    v += hb ? v2 : 0.f;
    v = sum_hi3(sum8(v));
    mean_b = bfr(mean);
    rden = __builtin_amdgcn_rcpf(bfr(__builtin_amdgcn_sqrtf(bfr(bfr(v * (1.f / DM)) + 1e-5f))));
  }
  SB();
  STEP(0); STEP(1);
  const bool dvalid = ((lane >> 3) & 1) == (lane >> 5);
  const int drow = w * 2 + (lane >> 5), dpos = c * 8 + ((lane >> 4) & 1) * 4;
  const int l16 = lane & 15;
  {
    v4u a1[5];
#pragma unroll
    for (int p = 0; p < 5; ++p) { const float xn = bfr(bflo(xr[p]) - mean_b) * rden; a1[p] = sel(cvtpk(xn, xn), msk); }
    SB();
    STEP(2); STEP(3); STEP(4);
    f32x4 acc = {0.f, 0.f, 0.f, 0.f};
#pragma unroll
    for (int p = 0; p < 5; ++p) acc = mfma(a1[p], R[p], acc);
    if (dvalid) *(f32x4*)&L.red1[drow * RS + dpos] = acc;
  }
  WG_BARRIER();
  asm volatile("" : "+s"(ep));
  const u64 tg = ep + 1;
  const unsigned tagA = (unsigned)tg, tagH = (unsigned)(tg >> 32), tagB = tagH & 0xffu;
  if (w >= 6 && has1) {
    const int pr = (w - 6) * 16 + l16;
    const f32x2 s = xw_sum<RS>(L.red1, pr, lane);
    if (lane < 16) __builtin_amdgcn_raw_buffer_store_b128(gran2(s[0], s[1], tagA, tagB), rs, WS_S1 + (cg * 2 + half) * 512 + pr * 16, 0, AUX_SC1);
  }
  SB();
  STAMP(2);
  STEP(5); STEP3(6); STEP3(9); STEP3(12);
  v4u g0, g1; unsigned b1p;
  const unsigned goff = WS_S1 + (tid < 96 ? (3 * h + (tid >> 5)) * 1024 + (tid & 31) * 16 : (96 + rg) * 1024 + (tid & 31) * 16);
  if (w < 2) {
    POLL_LD(g0, goff, ws, 0); POLL_LD(g1, goff, ws, 512);
    asm volatile("global_load_dword %0, %1, %2" : "=v"(b1p) : "v"(rg * 128 + (lane & 31) * 4), "s"(b1_) : "memory");
  }
  SB();
  STEP3(15);
  if (w < 2) {
    asm volatile("s_waitcnt vmcnt(3)" : "+v"(g0), "+v"(g1), "+v"(b1p) :: "memory");
    unsigned spins = 0;
    while (any_bad(gbad(g0, tagA, tagB) | gbad(g1, tagA, tagB))) {
      if (++spins > SPIN_CAP) break;
      __builtin_amdgcn_s_sleep(1);
      asm volatile("global_load_dwordx4 %0, %2, %3 sc1\n\tglobal_load_dwordx4 %1, %2, %3 offset:512 sc1\n\ts_waitcnt vmcnt(0)"
                   : "=&v"(g0), "=&v"(g1) : "v"(goff), "s"(ws) : "memory");
    }
    SETX(12, spins);
    float a0 = gval(g0[0]) + gval(g1[0]), a1 = gval(g0[2]) + gval(g1[2]);
    if (tid >= 96) {
      a0 = fmaxf(bfr(bfr(a0) + bflo(b1p)), 0.f); a1 = fmaxf(bfr(bfr(a1) + bfhi(b1p)), 0.f);
      a0 *= a0; a1 *= a1;
    }
    L.qh[tid] = cvtpk(a0, a1);
  }
  WG_BARRIER();
  STAMP(5);
  float sc[8];
  {
    v4u q[3];
#pragma unroll
    for (int i = 0; i < 3; ++i) q[i] = *(const v4u*)&L.qh[i * 32 + c * 4];
    const unsigned hv = ((const u16*)&L.qh[96])[w * 8 + r];
    constexpr float C2 = 0.07216878364870322f * 1.4426950408889634f;
#define QK(bk) do { float d_ = 0.f; _Pragma("unroll") for (int i = 0; i < 3; ++i) _Pragma("unroll") for (int k = 0; k < 4; ++k) d_ = dot2(R[I_K + 3 * (bk) + i][k], q[i][k], d_); \
    sc[bk] = sum8(d_) * C2; SB(); } while (0)
#define W2MM() do { const f32x4 acc_ = mfma(sel(hv | (hv << 16), msk), R[I_W2], f32x4{0.f, 0.f, 0.f, 0.f}); if (dvalid) *(f32x4*)&L.red2[drow * RS + dpos] = acc_; SB(); } while (0)
    STEP(18); QK(0); STEP(19); QK(1); STEP(20); QK(2); STEP(21); QK(3); STEP(22); QK(4); STEP(23); QK(5);
    STEP(24); W2MM(); STEP(25); STEP(26); QK(6); STEP3(27); QK(7);
  }
  {
    float m = sc[0];
#pragma unroll
    for (int bk = 1; bk < 8; ++bk) m = fmaxf(m, sc[bk]);
    m = max_hi3(m);
    float l = 0.f;
#pragma unroll
    for (int bk = 0; bk < 8; ++bk) l += __builtin_amdgcn_exp2f(sc[bk] - m);
    l = sum_hi3(l);
    if (lane == 0) { L.wm[w] = m; L.wl[w] = l; }
  }
  SB();
  STEP(I_V);
  WG_BARRIER();
  const float mw = L.wm[c], M = max8(mw);
  if (w == 5) {
    const float l = sum8(__builtin_amdgcn_exp2f(mw - M) * L.wl[c]);
    if (lane < 8) __builtin_amdgcn_raw_buffer_store_b128(v4u{asu(M), tagA, (asu(l) & 0xffffff00u) | tagB, tagA}, rs, WS_ML + (h * 8 + j) * 128 + lane * 16, 0, AUX_SC1);
  }
  SB();
  v4u A[8];
#pragma unroll
  for (int bk = 0; bk < 8; ++bk) { const float p = __builtin_amdgcn_exp2f(sc[bk] - M); A[bk] = sel(cvtpk(p, p), msk); }
  SB();
  STEP(I_V + 1);
  if (w >= 6 && b < 240) {
    const int pr = (w - 6) * 16 + l16;
    const f32x2 s = xw_sum<RS>(L.red2, pr, lane);
    if (lane < 16) __builtin_amdgcn_raw_buffer_store_b128(gran2(s[0], s[1], tagA, tagB), rs, WS_FIN + (cgrp * 4 + (pr >> 3)) * FIN_STRIDE + (48 + rg) * 128 + (pr & 7) * 16, 0, AUX_SC1);
  }
  SB();
  STAMP(6);
  float F;
  {
    v4u gm;
    const unsigned moff = WS_ML + (h * 8 + c) * 128 + r * 16;
    constexpr int NAFTER = (I_V + 18 + DEPTH < I_END) + (I_V + 19 + DEPTH < I_END) + (I_V + 20 + DEPTH < I_END);
    f32x4 acc[3] = {{0.f, 0.f, 0.f, 0.f}, {0.f, 0.f, 0.f, 0.f}, {0.f, 0.f, 0.f, 0.f}};
#define PV(i) do { acc[(i) % 3] = mfma(A[(i) / 3], R[I_V + (i)], acc[(i) % 3]); SB(); } while (0)
#define SPV(i) do { STEP(I_V + (i)); PV(i); } while (0)
#define SPV3(i) do { SPV(i); SPV((i) + 1); SPV((i) + 2); } while (0)
    PV(0); PV(1); SPV(2); SPV3(3); SPV3(6); SPV3(9); SPV3(12); SPV3(15);
    POLL_LD(gm, moff, ws, 0);
    SB();
    SPV3(18);
    asm volatile("s_waitcnt vmcnt(%1)" : "+v"(gm) : "n"(NAFTER) : "memory");
    {
      unsigned spins = 0;
      while (any_bad((gm[1] ^ tagA) | (gm[3] ^ tagA) | ((gm[2] ^ tagB) & 0xffu))) {
        if (++spins > SPIN_CAP) break;
        __builtin_amdgcn_s_sleep(2);
        asm volatile("global_load_dwordx4 %0, %1, %2 sc1\n\ts_waitcnt vmcnt(0)" : "=&v"(gm) : "v"(moff), "s"(ws) : "memory");
      }
      const float mc = asf(gm[0]), Mh = max8(mc);
      F = __builtin_amdgcn_exp2f(M - Mh) * __builtin_amdgcn_rcpf(sum8(__builtin_amdgcn_exp2f(mc - Mh) * gval(gm[2])));
    }
    SB();
    SPV3(21);
    if (dvalid) {
#pragma unroll
      for (int i = 0; i < 3; ++i) *(f32x4*)&L.redA[drow * RA + i * 64 + dpos] = acc[i];
    }
  }
  if constexpr (DEPTH < 4) ISSUE(I_L + 3);
  STAMP(7);
  WG_BARRIER();
  STAMP(3);
  if (w < 6) {
    const int pr = w * 16 + l16;
    const f32x2 s = xw_sum<RA>(L.redA, pr, lane);
    if (lane < 16) __builtin_amdgcn_raw_buffer_store_b128(gran2(s[0] * F, s[1] * F, tagA, tagB), rs, WS_OP + (h * 8 + j) * 1536 + pr * 16, 0, AUX_SC1);
  }
  SB();
  STAMP(4);
  if (!isred) {
    const int hh = 2 * P + (rowb >= DH ? 1 : 0), lr = rowb >= DH ? rowb - DH : rowb;
    const unsigned ooff = WS_OP + (hh * 8 + c) * 1536 + lr * 8 + r * 16;
    const bool hi = (lane >> 3) & 1;
    const v4u z4 = {0u, 0u, 0u, 0u}, mk0 = hi ? z4 : msk, mk1 = hi ? msk : z4;
    v4u go;
    unsigned spins = 0;
    for (;;) {
      asm volatile("global_load_dwordx4 %0, %1, %2 sc1\n\ts_waitcnt vmcnt(0)" : "=&v"(go) : "v"(ooff), "s"(ws) : "memory");
      if (!any_bad(gbad(go, tagA, tagB)) || ++spins > SPIN_CAP) break;
      __builtin_amdgcn_s_sleep(1);
    }
    STAMP(8);
    SETX(13, spins);
    const float o0 = sum8(gval(go[0])), o1 = sum8(gval(go[2]));
    const unsigned p0 = cvtpk(o0, o0), p1 = cvtpk(o1, o1);
    f32x4 acc = mfma(sel(p0, mk0), R[I_L], f32x4{0.f, 0.f, 0.f, 0.f});
    acc = mfma(sel(p1, mk0), R[I_L + 1], acc);
    acc = mfma(sel(p0, mk1), R[I_L + 2], acc);
    acc = mfma(sel(p1, mk1), R[I_L + 3], acc);
    *(f32x4*)&L.redB[drow * RB + l16 * 8 + ((lane >> 4) & 1) * 4] = acc;
    WG_BARRIER();
    if (w < 4) {
      const int pr = w * 16 + l16;
      const f32x2 s = xw_sum<RB>(L.redB, pr, lane);
      if (lane < 16) __builtin_amdgcn_raw_buffer_store_b128(gran2(s[0], s[1], tagA, tagB), rs, WS_FIN + (cg5 * 8 + (pr >> 3)) * FIN_STRIDE + (P * 3 + rgp) * 128 + (pr & 7) * 16, 0, AUX_SC1);
    }
    STAMP(9);
  } else if (P + 16 * w < 40) {
    const int fl = P + 16 * w;
    const unsigned xp = *(const GAS unsigned*)(x + fl * 32 + c * 4), b2p = *(const GAS unsigned*)((const GAS unsigned char*)b2_ + fl * 32 + c * 4);
    const unsigned foff = WS_FIN + fl * FIN_STRIDE + 4096 + lane * 16;
    v4u a[6];
    unsigned spins = 0;
    for (;;) {
      asm volatile("global_load_dwordx4 %0, %3, %4 offset:2048 sc1\n\tglobal_load_dwordx4 %1, %3, %4 offset:3072 sc1\n\tglobal_load_dwordx4 %2, %5, %4 sc1\n\ts_waitcnt vmcnt(0)"
                   : "=&v"(a[0]), "=&v"(a[1]), "=&v"(a[2]) : "v"(foff), "s"(ws), "v"(foff + 4096) : "memory");
      const unsigned bad = gbad(a[0], tagA, tagB) | gbad(a[1], tagA, tagB) | gbad(a[2], tagA, tagB);
      if (!any_bad(bad) || ++spins > SPIN_CAP) break;
      __builtin_amdgcn_s_sleep(1);
    }
    const float O0 = sum_hi3((gval(a[0][0]) + gval(a[1][0])) + gval(a[2][0])), O1 = sum_hi3((gval(a[0][2]) + gval(a[1][2])) + gval(a[2][2]));
    const float t0 = bfr(bfr(O0) + bflo(b2p)), t1 = bfr(bfr(O1) + bfhi(b2p));
    constexpr float L2E = 1.4426950408889634f;
    float ga = bfr(t0 * __builtin_amdgcn_rcpf(1.f + __builtin_amdgcn_exp2f(-L2E * t0)));
    float gb = bfr(t1 * __builtin_amdgcn_rcpf(1.f + __builtin_amdgcn_exp2f(-L2E * t1)));
    asm volatile("" : "+v"(ga), "+v"(gb));
    STAMP(9);
    spins = 0;
    for (;;) {
      asm volatile("global_load_dwordx4 %0, %6, %7 offset:-4096 sc1\n\tglobal_load_dwordx4 %1, %6, %7 offset:-3072 sc1\n\tglobal_load_dwordx4 %2, %6, %7 offset:-2048 sc1\n\t"
                   "global_load_dwordx4 %3, %6, %7 offset:-1024 sc1\n\tglobal_load_dwordx4 %4, %6, %7 sc1\n\tglobal_load_dwordx4 %5, %6, %7 offset:1024 sc1\n\ts_waitcnt vmcnt(0)"
                   : "=&v"(a[0]), "=&v"(a[1]), "=&v"(a[2]), "=&v"(a[3]), "=&v"(a[4]), "=&v"(a[5]) : "v"(foff), "s"(ws) : "memory");
      unsigned bad = 0;
#pragma unroll
      for (int i = 0; i < 6; ++i) bad |= gbad(a[i], tagA, tagB);
      if (!any_bad(bad) || ++spins > SPIN_CAP) break;
      __builtin_amdgcn_s_sleep(1);
    }
    STAMP(10);
    SETX(14, spins);
    const float S0 = sum_hi3(((gval(a[0][0]) + gval(a[1][0])) + (gval(a[2][0]) + gval(a[3][0]))) + (gval(a[4][0]) + gval(a[5][0])));
    const float S1 = sum_hi3(((gval(a[0][2]) + gval(a[1][2])) + (gval(a[2][2]) + gval(a[3][2]))) + (gval(a[4][2]) + gval(a[5][2])));
    const float y0 = bflo(xp) + bfr(bfr(S0) * ga), y1 = bfhi(xp) + bfr(bfr(S1) * gb);
    if (lane < 8) *(GAS unsigned*)((GAS unsigned char*)out_ + fl * 32 + c * 4) = cvtpk(y0, y1);
    if (fl == 0 && lane == 0) __hip_atomic_store((GAS u64*)(ws + WS_EPOCH), ((u64)tagH << 32) | tagA, __ATOMIC_RELAXED, __HIP_MEMORY_SCOPE_AGENT);
    STAMP(11);
  }
  STAMP_FLUSH();
}

extern "C" void kernel_launch(void* const* d_in, const int* in_sizes, int n_in, void* d_out, int out_size, void* d_ws, size_t ws_size,
                              hipStream_t stream) {
  (void)in_sizes; (void)n_in; (void)out_size; (void)ws_size;
  fused_decode_kernel<<<NWG, NTHR, 0, stream>>>((const u16*)d_in[0], (const u16*)d_in[1], (const u16*)d_in[2], (const u16*)d_in[3],
                                                (const u16*)d_in[4], (const u16*)d_in[6], (const u16*)d_in[7], (const u16*)d_in[8],
                                                (const u16*)d_in[9], (u16*)d_out, (unsigned char*)d_ws);
}
```

```cpp
#include <hip/hip_runtime.h>
#include <hip/hip_bf16.h>
#include <stdint.h>

#define GAS __attribute__((address_space(1)))
#define LAS __attribute__((address_space(3)))
#define LP(T, a) ((LAS T*)(size_t)(a))
typedef unsigned short u16;
typedef unsigned long long u64;
typedef unsigned v4u __attribute__((ext_vector_type(4)));
typedef float f32x4 __attribute__((ext_vector_type(4)));
typedef float f32x2 __attribute__((ext_vector_type(2)));
typedef short bf16x8 __attribute__((ext_vector_type(8)));
typedef __bf16 bf16x2 __attribute__((ext_vector_type(2)));

constexpr int DM = 640, NH = 32, DH = 192, SK = 32768, WIN = 4096, FF = 1536;
constexpr int NWG = 256, NTHR = 512;
constexpr int QN = NH * DH;

constexpr int WS_EPOCH = 0;
constexpr int WS_S1 = 4096;
constexpr int WS_ML = WS_S1 + 240 * 64 * 8;
constexpr int WS_OP = WS_ML + 256 * 128;
constexpr int FIN_STRIDE = 72 * 128;
constexpr int WS_FIN = WS_OP + 256 * 192 * 8;
constexpr int WS_END = WS_FIN + 40 * FIN_STRIDE;
#define STAMP(i) do { } while (0)
#define STAMP0() do { } while (0)
#define STAMP_DECL do { } while (0)
#define SETX(i, v) do { } while (0)
#define STAMP_FLUSH() do { } while (0)

constexpr int AUX_SC1 = 16;
#define DEPTH 3
#define WO_LAST
#define SLEEP1 "1"
#define STAG2 "5"
#define TSLEEP 1
#define TAIL_SLEEP() do { if (TSLEEP) __builtin_amdgcn_s_sleep(TSLEEP); } while (0)
constexpr unsigned SPIN_CAP = 1u << 18;

#define SB() __builtin_amdgcn_sched_barrier(0)
#define CFENCE() asm volatile("" ::: "memory")
#define WG_BARRIER() do { asm volatile("s_waitcnt lgkmcnt(0)" ::: "memory"); __builtin_amdgcn_s_barrier(); asm volatile("" ::: "memory"); } while (0)

#define AUX_STREAM 2
typedef __amdgpu_buffer_rsrc_t rsrc_t;
__device__ __forceinline__ rsrc_t mkrs(const void* p, unsigned bytes) { return __builtin_amdgcn_make_buffer_rsrc((void*)p, 0, bytes, 0x00020000); }
__device__ __forceinline__ v4u ldst(rsrc_t rs, unsigned voff, unsigned soff) { return __builtin_amdgcn_raw_buffer_load_b128(rs, voff, soff, AUX_STREAM); }
#define POLL_LD(dst, voff, base, imm) asm volatile("global_load_dwordx4 %0, %1, %2 offset:" #imm " sc1" : "=v"(dst) : "v"(voff), "s"(base) : "memory")
__device__ __forceinline__ float dot2(unsigned k, unsigned q, float acc) {
  return __builtin_amdgcn_fdot2_f32_bf16(__builtin_bit_cast(bf16x2, k), __builtin_bit_cast(bf16x2, q), acc, false);
}
__device__ __forceinline__ unsigned cvtpk(float lo, float hi) {
  bf16x2 v = {(__bf16)lo, (__bf16)hi};
  return __builtin_bit_cast(unsigned, v);
}
__device__ __forceinline__ float asf(unsigned u) { return __builtin_bit_cast(float, u); }
__device__ __forceinline__ unsigned asu(float f) { return __builtin_bit_cast(unsigned, f); }
__device__ __forceinline__ float bfr(float f) { return asf(cvtpk(f, f) << 16); }
__device__ __forceinline__ float bflo(unsigned u) { return asf(u << 16); }
__device__ __forceinline__ float bfhi(unsigned u) { return asf(u & 0xffff0000u); }
template <int CTRL> __device__ __forceinline__ float dpp(float x) {
  return __builtin_bit_cast(float, __builtin_amdgcn_mov_dpp(__builtin_bit_cast(int, x), CTRL, 0xf, 0xf, true));
}
constexpr int XOR1 = 0xB1, XOR2 = 0x4E, HMIR = 0x141, ROR8 = 0x128, ROR4 = 0x124;
__device__ __forceinline__ float sum8(float x) { x += dpp<XOR1>(x); x += dpp<XOR2>(x); x += dpp<HMIR>(x); return x; }
__device__ __forceinline__ float max8(float x) { x = fmaxf(x, dpp<XOR1>(x)); x = fmaxf(x, dpp<XOR2>(x)); x = fmaxf(x, dpp<HMIR>(x)); return x; }
__device__ __forceinline__ float sum_hi3(float x) {
  x += dpp<ROR8>(x);
  auto s = __builtin_amdgcn_permlane16_swap(asu(x), asu(x), false, false);
  x = asf(s[0]) + asf(s[1]);
  auto t = __builtin_amdgcn_permlane32_swap(asu(x), asu(x), false, false);
  return asf(t[0]) + asf(t[1]);
}
__device__ __forceinline__ float max_hi3(float x) {
  x = fmaxf(x, dpp<ROR8>(x));
  auto s = __builtin_amdgcn_permlane16_swap(asu(x), asu(x), false, false);
  x = fmaxf(asf(s[0]), asf(s[1]));
  auto t = __builtin_amdgcn_permlane32_swap(asu(x), asu(x), false, false);
  return fmaxf(asf(t[0]), asf(t[1]));
}
__device__ __forceinline__ f32x4 mfma(v4u a, v4u b, f32x4 c) {
  return __builtin_amdgcn_mfma_f32_16x16x32_bf16(__builtin_bit_cast(bf16x8, a), __builtin_bit_cast(bf16x8, b), c, 0, 0, 0);
}
__device__ __forceinline__ v4u sel(unsigned pp, const v4u& m) { return v4u{pp & m[0], pp & m[1], pp & m[2], pp & m[3]}; }
template <class T> __device__ __forceinline__ unsigned ldsaddr(T* p) { return (unsigned)(size_t)(LAS T*)p; }
template <int ROW> __device__ __forceinline__ f32x2 xw_sum(unsigned p) {
  const f32x2 s = (*LP(const f32x2, p) + *LP(const f32x2, p + ROW * 4)) + (*LP(const f32x2, p + ROW * 8) + *LP(const f32x2, p + ROW * 12));
  auto a = __builtin_amdgcn_permlane16_swap(asu(s[0]), asu(s[0]), false, false);
  auto c = __builtin_amdgcn_permlane16_swap(asu(s[1]), asu(s[1]), false, false);
  const float u = asf(a[0]) + asf(a[1]), v = asf(c[0]) + asf(c[1]);
  auto d = __builtin_amdgcn_permlane32_swap(asu(u), asu(u), false, false);
  auto e = __builtin_amdgcn_permlane32_swap(asu(v), asu(v), false, false);
  return f32x2{asf(d[0]) + asf(d[1]), asf(e[0]) + asf(e[1])};
}
__device__ __forceinline__ v4u gran2(float v0, float v1, unsigned tagA, unsigned tagB) {
  return v4u{(asu(v0) & 0xffffff00u) | tagB, tagA, (asu(v1) & 0xffffff00u) | tagB, tagA};
}
__device__ __forceinline__ unsigned gbad(const v4u& g, unsigned tagA, unsigned tagB) {
  return (g[1] ^ tagA) | (g[3] ^ tagA) | (((g[0] ^ tagB) | (g[2] ^ tagB)) & 0xffu);
}
__device__ __forceinline__ bool any_bad(unsigned bad) { return __builtin_amdgcn_ballot_w64(bad != 0u) != 0ull; }
__device__ __forceinline__ float gval(unsigned u) { return asf(u & 0xffffff00u); }

constexpr int I_W2 = 5, I_K = 6, I_V = 30, I_L = 54, I_END = 58;
#define D_INIT DEPTH
#define D_BAR DEPTH
#define D_END 4
constexpr int depth_at(int i) { return i < 0 ? D_INIT : (i == 4 || i == 17 || i == I_V) ? D_BAR : i >= I_END - 5 - 1 ? D_END : DEPTH; }
constexpr int upto(int i) { int u = -1; for (int k = -1; k <= i; ++k) { const int t = k + depth_at(k) < I_END - 1 ? k + depth_at(k) : I_END - 1; u = t > u ? t : u; } return u; }
constexpr int RA = 200, RB = 136, RS = 72;
struct alignas(16) Lds {
  float redA[16 * RA];
  float redB[16 * RB];
  float red1[16 * RS];
  float red2[16 * RS];
  unsigned qh[128];
  float wm[8];
  float wl[8];
  u16 xs[640];
};

__global__ void __launch_bounds__(NTHR, 2)
fused_decode_kernel(const u16*, const u16*, const u16*, const u16*, const u16*, const u16*, const u16*, const u16*, const u16*, u16*, unsigned char*) {
  typedef unsigned v16u __attribute__((ext_vector_type(16)));
  v16u ka; v4u kb; u64 kw;
  int b = blockIdx.x;
  asm volatile("s_load_dwordx16 %0, %4, 0x0\n\ts_load_dwordx4 %1, %4, 0x40\n\ts_load_dwordx2 %2, %4, 0x50"
               : "=&s"(ka), "=&s"(kb), "=&s"(kw), "+s"(b) : "s"(__builtin_amdgcn_kernarg_segment_ptr()));
  SB();
  __shared__ Lds L;
  STAMP_DECL;
  STAMP0();
  asm volatile("" ::: "v187");
  const int tid = threadIdx.x, lane = tid & 63, w = __builtin_amdgcn_readfirstlane(tid >> 6), r = lane >> 3, c = lane & 7;
  const int h = (b & 7) * 4 + ((b >> 3) & 3), j = b >> 5;
  const bool isq = j < 6;
  const int u1 = (j == 6) ? h : 32 + h;
  const bool has1 = isq || u1 < 48;
  const int mq = (j - 6) >> 31;
  int cg = ((3 * h + (j >> 1)) & mq) | ((96 + (u1 >> 1)) & ~mq), half = ((j & mq) | (u1 & ~mq)) & 1;
  unsigned stride = FF * 2 + ((QN * 2 - FF * 2) & mq);
  unsigned o1 = (cg - (96 & ~mq)) * 128, n1 = has1 ? DM * stride : 0u;
  unsigned off1 = (half * 320 + w * 8 + r) * stride + c * 16;
  unsigned kvrow = h * SK + (SK - WIN) + (w * 8 + j) * 8;
  constexpr int BSTR = 512 * DH * 2;
  u64 kvoff = (u64)kvrow * (DH * 2);
  unsigned loff = (unsigned)(r * (DH * 2) + c * 16);
  const int vu = b < 240 ? b : 239;
  int rg = vu / 10, cgrp = vu - rg * 10;
  unsigned o2 = (rg * 64 + w * 8) * (DM * 2) + cgrp * 128;
  const int P = h >> 1, jj = (h & 1) * 8 + j;
  int rgp = jj / 5, cg5 = jj - rgp * 5;
  const bool isred = jj == 15;
  const int rowb = 128 * rgp + 16 * w;
  unsigned ol = (P * 384 + rowb) * (DM * 2) + cg5 * 256;
  unsigned offl = (lane >> 4) * (4 * DM * 2) + (lane & 15) * 16, off2 = r * (DM * 2) + c * 16, offx = lane * 16, offx2 = (lane & 15) * 16;
  asm volatile("" : "+s"(cg), "+s"(half), "+s"(stride), "+s"(o1), "+s"(n1), "+s"(kvoff), "+s"(rg), "+s"(cgrp), "+s"(o2), "+s"(rgp), "+s"(cg5), "+s"(ol));
  asm volatile("" : "+v"(off1), "+v"(loff), "+v"(offl), "+v"(off2), "+v"(offx), "+v"(offx2));
  SB();
  asm volatile("s_waitcnt lgkmcnt(0)" : "+s"(ka), "+s"(kb), "+s"(kw));
#define KARG(v, i) ((const unsigned char*)(((u64)(v)[2 * (i) + 1] << 32) | (v)[2 * (i)]))
  const unsigned char *x_ = KARG(ka, 0), *kc_ = KARG(ka, 1), *vc_ = KARG(ka, 2), *wq_ = KARG(ka, 3), *wo_ = KARG(ka, 4), *w1_ = KARG(ka, 5), *b1_ = KARG(ka, 6),
                      *w2_ = KARG(ka, 7), *b2_ = KARG(kb, 0), *out_ = KARG(kb, 1), *ws_ = (const unsigned char*)kw;
  GAS unsigned char* ws = (GAS unsigned char*)ws_;
  const GAS unsigned char* x = (const GAS unsigned char*)x_;
  const rsrc_t rw1 = mkrs((const unsigned char*)(isq ? wq_ : w1_) + o1, n1);
  rsrc_t RW2, RWO, RK, RV;
  v4u R[I_END];
#define ISSUE(i) do { if constexpr ((i) < I_W2) R[i] = ldst(rw1, off1, (i) * 64 * stride); \
    else if constexpr ((i) == I_W2) R[i] = ldst(RW2, off2, 0); \
    else if constexpr ((i) >= I_K && (i) < I_K + 24) R[i] = ldst(RK, loff + (((i) - I_K) % 3) * 128, (((i) - I_K) / 3) * BSTR); \
    else if constexpr ((i) >= I_V && (i) < I_V + 24) R[i] = ldst(RV, loff + (((i) - I_V) % 3) * 128, (((i) - I_V) / 3) * BSTR); \
    else if constexpr ((i) >= I_L && (i) < I_L + 4) R[i] = ldst(RWO, offl + ((i) - I_L) * (DM * 2), 0); \
    SB(); } while (0)
#define TOUCH(i) do { asm volatile("" : "+v"(R[i])); SB(); } while (0)
#define STEP(i) do { constexpr int u0_ = upto((i) - 1), u1_ = upto(i); TOUCH(i); if constexpr (u0_ < u1_) ISSUE(u0_ + 1); if constexpr (u0_ + 1 < u1_) ISSUE(u0_ + 2); \
    if constexpr (u0_ + 2 < u1_) ISSUE(u0_ + 3); } while (0)
#define STEP3(i) do { STEP(i); STEP((i) + 1); STEP((i) + 2); } while (0)

  const v4u xa = *(const GAS v4u*)(x + offx);
  const v4u xb = *(const GAS v4u*)(x + 1024 + offx2);
  SB();
  ISSUE(0); ISSUE(1);
  if constexpr (upto(-1) >= 2) ISSUE(2);
  if constexpr (upto(-1) >= 3) ISSUE(3);
  static_assert(upto(-1) <= 3 && upto(I_V + 23) == I_END - 1, "issue schedule");
  u64 ep;
  asm volatile("s_load_dwordx2 %0, %1, 0x0" : "=s"(ep) : "s"(ws_) : "memory");
  SB();
  STAMP(1);
  SETX(15, (unsigned)__builtin_amdgcn_s_getreg(20 | (3 << 11)) & 0xfu);
  const rsrc_t rs = mkrs(ws_, WS_END);
  RW2 = mkrs((const unsigned char*)w2_ + o2, b < 240 ? 8 * DM * 2 : 0);
  RWO = mkrs((const unsigned char*)wo_ + ol, isred ? 0 : 16 * DM * 2);
  RK = mkrs((const unsigned char*)kc_ + kvoff, 7 * BSTR + 8 * DH * 2);
  RV = mkrs((const unsigned char*)vc_ + kvoff, 7 * BSTR + 8 * DH * 2);

  v4u msk;
#pragma unroll
  for (int d = 0; d < 4; ++d) msk[d] = ((c >> 1) == d) ? ((c & 1) ? 0xffff0000u : 0x0000ffffu) : 0u;

  STEP(0); STEP(1);
  if constexpr (D_INIT >= 3) STEP(2);
  if constexpr (D_INIT >= 4) STEP(3);
  float mean_b, rden;
  unsigned xr[5];
  {
    const bool hb = lane < 16;
    *(v4u*)&L.xs[lane * 8] = xa;
    if (hb) *(v4u*)&L.xs[512 + lane * 8] = xb;
#pragma unroll
    for (int p = 0; p < 5; ++p) xr[p] = L.xs[half * 320 + p * 64 + w * 8 + r];
    float e[16];
#pragma unroll
    for (int k = 0; k < 4; ++k) { e[2 * k] = bflo(xa[k]); e[2 * k + 1] = bfhi(xa[k]); e[8 + 2 * k] = bflo(xb[k]); e[9 + 2 * k] = bfhi(xb[k]); }
    float s = 0.f, s2 = 0.f;
#pragma unroll
    for (int k = 0; k < 8; ++k) { s += e[k]; s2 += e[8 + k]; }
    s += hb ? s2 : 0.f;
    s = sum_hi3(sum8(s));
    const float mean = s * (1.f / DM);
    float v = 0.f, v2 = 0.f;
#pragma unroll
    for (int k = 0; k < 8; ++k) { const float d0 = e[k] - mean, d1 = e[8 + k] - mean; v = fmaf(d0, d0, v); v2 = fmaf(d1, d1, v2); }
    v += hb ? v2 : 0.f;
    v = sum_hi3(sum8(v));
    mean_b = bfr(mean);
    rden = __builtin_amdgcn_rcpf(bfr(__builtin_amdgcn_sqrtf(bfr(bfr(v * (1.f / DM)) + 1e-5f))));
  }
  SB();
#define PRE_STEPS() do { } while (0)
#define MID_STEPS() do { if constexpr (D_INIT < 3) STEP(2); if constexpr (D_INIT < 4) STEP(3); } while (0)
  const bool dvalid = ((lane >> 3) & 1) == (lane >> 5);
  const int drow = w * 2 + (lane >> 5), dpos = c * 8 + ((lane >> 4) & 1) * 4;
  const int l16 = lane & 15;
  {
    v4u a1[5];
#pragma unroll
    for (int p = 0; p < 5; ++p) { const float xn = bfr(bflo(xr[p]) - mean_b) * rden; a1[p] = sel(cvtpk(xn, xn), msk); }
    SB();
    MID_STEPS(); STEP(4);
    f32x4 acc = {0.f, 0.f, 0.f, 0.f};
#pragma unroll
    for (int p = 0; p < 5; ++p) acc = mfma(a1[p], R[p], acc);
    if (dvalid) *(f32x4*)&L.red1[drow * RS + dpos] = acc;
  }
  WG_BARRIER();
  asm volatile("" : "+s"(ep));
  const u64 tg = ep + 1;
  const unsigned tagA = (unsigned)tg, tagH = (unsigned)(tg >> 32), tagB = tagH & 0xffu;
  if (w >= 6 && has1) {
    const int pr = (w - 6) * 16 + l16;
    const f32x2 s = xw_sum<RS>(ldsaddr(L.red1) + ((lane >> 4) * (4 * RS) + 2 * pr) * 4);
    if (lane < 16) __builtin_amdgcn_raw_buffer_store_b128(gran2(s[0], s[1], tagA, tagB), rs, WS_S1 + (cg * 2 + half) * 512 + pr * 16, 0, AUX_SC1);
  }
  SB();
  STAMP(2);
  STEP(5); STEP3(6); STEP3(9); STEP3(12);
  v4u g0, g1; unsigned b1p;
  const unsigned goff = WS_S1 + (tid < 96 ? (3 * h + (tid >> 5)) * 1024 + (tid & 31) * 16 : (96 + rg) * 1024 + (tid & 31) * 16);
  if (w < 2) {
    POLL_LD(g0, goff, ws, 0); POLL_LD(g1, goff, ws, 512);
    asm volatile("global_load_dword %0, %1, %2" : "=v"(b1p) : "v"(rg * 128 + (lane & 31) * 4), "s"(b1_) : "memory");
  }
  SB();
  STEP3(15);
  if (w < 2) {
    asm volatile("s_waitcnt vmcnt(%3)" : "+v"(g0), "+v"(g1), "+v"(b1p) : "n"(upto(17) - upto(14)) : "memory");
    unsigned spins = 0;
    while (any_bad(gbad(g0, tagA, tagB) | gbad(g1, tagA, tagB))) {
      if (++spins > SPIN_CAP) break;
      __builtin_amdgcn_s_sleep(1);
      asm volatile("global_load_dwordx4 %0, %2, %3 sc1\n\tglobal_load_dwordx4 %1, %2, %3 offset:512 sc1\n\ts_waitcnt vmcnt(0)"
                   : "=&v"(g0), "=&v"(g1) : "v"(goff), "s"(ws) : "memory");
    }
    SETX(12, spins);
    float a0 = gval(g0[0]) + gval(g1[0]), a1 = gval(g0[2]) + gval(g1[2]);
    if (tid >= 96) {
      a0 = fmaxf(bfr(bfr(a0) + bflo(b1p)), 0.f); a1 = fmaxf(bfr(bfr(a1) + bfhi(b1p)), 0.f);
      a0 *= a0; a1 *= a1;
    }
    L.qh[tid] = cvtpk(a0, a1);
  }
  WG_BARRIER();
  STAMP(5);
  float sc[8];
  {
    v4u q[3];
#pragma unroll
    for (int i = 0; i < 3; ++i) q[i] = *(const v4u*)&L.qh[i * 32 + c * 4];
    const unsigned hv = ((const u16*)&L.qh[96])[w * 8 + r];
    constexpr float C2 = 0.07216878364870322f * 1.4426950408889634f;
#define QK(bk) do { float d_ = 0.f; _Pragma("unroll") for (int i = 0; i < 3; ++i) _Pragma("unroll") for (int k = 0; k < 4; ++k) d_ = dot2(R[I_K + 3 * (bk) + i][k], q[i][k], d_); \
    sc[bk] = sum8(d_) * C2; SB(); } while (0)
#define W2MM() do { const f32x4 acc_ = mfma(sel(hv | (hv << 16), msk), R[I_W2], f32x4{0.f, 0.f, 0.f, 0.f}); if (dvalid) *(f32x4*)&L.red2[drow * RS + dpos] = acc_; SB(); } while (0)
    STEP(18); QK(0); STEP(19); QK(1); STEP(20); QK(2); STEP(21); QK(3); STEP(22); QK(4); STEP(23); QK(5);
    STEP(24); W2MM(); STEP(25); STEP(26); QK(6); STEP3(27); QK(7);
  }
  {
    float m = sc[0];
#pragma unroll
    for (int bk = 1; bk < 8; ++bk) m = fmaxf(m, sc[bk]);
    m = max_hi3(m);
    float l = 0.f;
#pragma unroll
    for (int bk = 0; bk < 8; ++bk) l += __builtin_amdgcn_exp2f(sc[bk] - m);
    l = sum_hi3(l);
    if (lane == 0) { L.wm[w] = m; L.wl[w] = l; }
  }
  SB();
  STEP(I_V);
  WG_BARRIER();
  const float mw = L.wm[c], M = max8(mw);
  if (w == 5) {
    const float l = sum8(__builtin_amdgcn_exp2f(mw - M) * L.wl[c]);
    if (lane < 8) __builtin_amdgcn_raw_buffer_store_b128(v4u{asu(M), tagA, (asu(l) & 0xffffff00u) | tagB, tagA}, rs, WS_ML + (h * 8 + j) * 128 + lane * 16, 0, AUX_SC1);
  }
  SB();
  v4u A[8];
#pragma unroll
  for (int bk = 0; bk < 8; ++bk) { const float p = __builtin_amdgcn_exp2f(sc[bk] - M); A[bk] = sel(cvtpk(p, p), msk); }
  SB();
  STEP(I_V + 1);
  if (w >= 6 && b < 240) {
    const int pr = (w - 6) * 16 + l16;
    const f32x2 s = xw_sum<RS>(ldsaddr(L.red2) + ((lane >> 4) * (4 * RS) + 2 * pr) * 4);
    if (lane < 16) __builtin_amdgcn_raw_buffer_store_b128(gran2(s[0], s[1], tagA, tagB), rs, WS_FIN + (cgrp * 4 + (pr >> 3)) * FIN_STRIDE + (48 + rg) * 128 + (pr & 7) * 16, 0, AUX_SC1);
  }
  SB();
  STAMP(6);
  float F;
  const int prw = w * 16 + l16;
  unsigned aA = ldsaddr(L.redA) + ((lane >> 4) * (4 * RA) + 2 * prw) * 4, aAw = ldsaddr(L.redA) + (drow * RA + dpos) * 4;
  unsigned aB = ldsaddr(L.redB) + ((lane >> 4) * (4 * RB) + 2 * prw) * 4, aBw = ldsaddr(L.redB) + (drow * RB + l16 * 8 + ((lane >> 4) & 1) * 4) * 4;
  unsigned sA = WS_OP + (h * 8 + j) * 1536 + prw * 16;
  unsigned sB = WS_FIN + (cg5 * 8 + (prw >> 3)) * FIN_STRIDE + (P * 3 + rgp) * 128 + (prw & 7) * 16;
  unsigned ooff = WS_OP + ((2 * P + (rowb >= DH ? 1 : 0)) * 8 + c) * 1536 + (rowb >= DH ? rowb - DH : rowb) * 8 + r * 16;
  unsigned vff = 0xffu;
  const v4u z4 = {0u, 0u, 0u, 0u};
  v4u mk0 = ((lane >> 3) & 1) ? z4 : msk, mk1 = ((lane >> 3) & 1) ? msk : z4;
  {
    v4u gm;
    const unsigned moff = WS_ML + (h * 8 + c) * 128 + r * 16;
    constexpr int NAFTER = upto(I_V + 20) - upto(I_V + 17);
    f32x4 acc[3] = {{0.f, 0.f, 0.f, 0.f}, {0.f, 0.f, 0.f, 0.f}, {0.f, 0.f, 0.f, 0.f}};
#define PV(i) do { acc[(i) % 3] = mfma(A[(i) / 3], R[I_V + (i)], acc[(i) % 3]); SB(); } while (0)
#define SPV(i) do { STEP(I_V + (i)); PV(i); } while (0)
#define SPV3(i) do { SPV(i); SPV((i) + 1); SPV((i) + 2); } while (0)
    PV(0); PV(1); SPV(2); SPV3(3); SPV3(6); SPV3(9); SPV3(12); SPV3(15);
    POLL_LD(gm, moff, ws, 0);
    SB();
    SPV3(18);
    asm volatile("s_waitcnt vmcnt(%1)" : "+v"(gm) : "n"(NAFTER) : "memory");
    {
      unsigned spins = 0;
      while (any_bad((gm[1] ^ tagA) | (gm[3] ^ tagA) | ((gm[2] ^ tagB) & 0xffu))) {
        if (++spins > SPIN_CAP) break;
        __builtin_amdgcn_s_sleep(2);
        asm volatile("global_load_dwordx4 %0, %1, %2 sc1\n\ts_waitcnt vmcnt(0)" : "=&v"(gm) : "v"(moff), "s"(ws) : "memory");
      }
      const float mc = asf(gm[0]), Mh = max8(mc);
      F = __builtin_amdgcn_exp2f(M - Mh) * __builtin_amdgcn_rcpf(sum8(__builtin_amdgcn_exp2f(mc - Mh) * gval(gm[2])));
    }
    asm volatile("" : "+v"(F), "+v"(aA), "+v"(aAw), "+v"(sA), "+v"(ooff), "+v"(aB), "+v"(aBw), "+v"(sB), "+v"(mk0), "+v"(mk1), "+v"(vff));
    SB();
    SPV3(21);
    if (dvalid) {
#pragma unroll
      for (int i = 0; i < 3; ++i) *LP(f32x4, aAw + i * 256) = acc[i];
    }
  }
  STAMP(7);
  SETX(14, __builtin_amdgcn_s_memtime() - cyc0);
  WG_BARRIER();
  STAMP(3);
  if (w < 6) {
    const f32x2 s = xw_sum<RA>(aA);
    if (lane < 16) __builtin_amdgcn_raw_buffer_store_b128(gran2(s[0] * F, s[1] * F, tagA, tagB), rs, sA, 0, AUX_SC1);
  }
  SB();
  STAMP(4);
  if (!isred) {
    float o0, o1;
    unsigned spins = SPIN_CAP;
    u64 sv;
    asm volatile(
      "s_mov_b64 %[sv], exec\n\t"
      "1:\n\t"
      "global_load_dwordx4 v[128:131], %[off], %[base] sc1\n\t"
      "s_waitcnt vmcnt(0)\n\t"
      "v_bitop3_b32 v184, v129, v131, %[ta] bitop3:0x7e\n\tv_bitop3_b32 v185, v128, v130, %[tb] bitop3:0x7e\n\t"
      "v_and_or_b32 v184, v185, %[ff], v184\n\tv_cmp_ne_u32 vcc, 0, v184\n\t"
      "s_and_b64 exec, exec, vcc\n\t"
      "s_cbranch_scc0 2f\n\t"
      "s_sleep " SLEEP1 "\n\t"
      "s_sub_u32 %[n], %[n], 1\n\ts_cbranch_scc0 1b\n\t"
      "2:\n\t"
      "s_mov_b64 exec, %[sv]\n\t"
      "v_and_b32 %[o0], %[mk], v128\n\tv_and_b32 %[o1], %[mk], v130\n\ts_nop 1"
      : [o0] "=&v"(o0), [o1] "=&v"(o1), [n] "+s"(spins), [sv] "=&s"(sv)
      : [off] "v"(ooff), [base] "s"(ws), [ta] "s"(tagA), [tb] "s"(tagB), [ff] "v"(vff), [mk] "s"(0xffffff00u)
      : "memory", "vcc", "scc", "v128", "v129", "v130", "v131", "v184", "v185");
    STAMP(8);
    SETX(13, SPIN_CAP - spins);
    o0 = sum8(o0); o1 = sum8(o1);
    const unsigned p0 = cvtpk(o0, o0), p1 = cvtpk(o1, o1);
    f32x4 acc = mfma(sel(p0, mk0), R[I_L], f32x4{0.f, 0.f, 0.f, 0.f});
    acc = mfma(sel(p1, mk0), R[I_L + 1], acc);
    acc = mfma(sel(p0, mk1), R[I_L + 2], acc);
    acc = mfma(sel(p1, mk1), R[I_L + 3], acc);
    *LP(f32x4, aBw) = acc;
    WG_BARRIER();
    if (w < 4) {
      const f32x2 s = xw_sum<RB>(aB);
      if (lane < 16) __builtin_amdgcn_raw_buffer_store_b128(gran2(s[0], s[1], tagA, tagB), rs, sB, 0, AUX_SC1);
    }
    STAMP(9);
  } else if (P + 16 * w < 40) {
    const int fl = P + 16 * w;
    const unsigned xp = *(const GAS unsigned*)(x + fl * 32 + c * 4), b2p = *(const GAS unsigned*)((const GAS unsigned char*)b2_ + fl * 32 + c * 4);
    const unsigned foff = WS_FIN + fl * FIN_STRIDE + 4096 + lane * 16;
    v4u a[6];
    unsigned spins = 0;
    for (;;) {
      asm volatile("global_load_dwordx4 %0, %3, %4 offset:2048 sc1\n\tglobal_load_dwordx4 %1, %3, %4 offset:3072 sc1\n\tglobal_load_dwordx4 %2, %5, %4 sc1\n\ts_waitcnt vmcnt(0)"
                   : "=&v"(a[0]), "=&v"(a[1]), "=&v"(a[2]) : "v"(foff), "s"(ws), "v"(foff + 4096) : "memory");
      const unsigned bad = gbad(a[0], tagA, tagB) | gbad(a[1], tagA, tagB) | gbad(a[2], tagA, tagB);
      if (!any_bad(bad) || ++spins > SPIN_CAP) break;
      __builtin_amdgcn_s_sleep(1);
    }
    const float O0 = sum_hi3((gval(a[0][0]) + gval(a[1][0])) + gval(a[2][0])), O1 = sum_hi3((gval(a[0][2]) + gval(a[1][2])) + gval(a[2][2]));
    const float t0 = bfr(bfr(O0) + bflo(b2p)), t1 = bfr(bfr(O1) + bfhi(b2p));
    constexpr float L2E = 1.4426950408889634f;
    float ga = bfr(t0 * __builtin_amdgcn_rcpf(1.f + __builtin_amdgcn_exp2f(-L2E * t0)));
    float gb = bfr(t1 * __builtin_amdgcn_rcpf(1.f + __builtin_amdgcn_exp2f(-L2E * t1)));
    GAS unsigned* op = (GAS unsigned*)((GAS unsigned char*)out_ + fl * 32 + c * 4);
    float x0 = bflo(xp), x1 = bfhi(xp);
    asm volatile("" : "+v"(ga), "+v"(gb), "+v"(op), "+v"(x0), "+v"(x1));
    STAMP(9);
    float sa[6], sb[6];
    spins = SPIN_CAP;
    asm volatile(
      "global_load_dwordx4 v[128:131], %[off], %[base] offset:-4096 sc1\n\t"
      "global_load_dwordx4 v[132:135], %[off], %[base] offset:-3072 sc1\n\t"
      "global_load_dwordx4 v[136:139], %[off], %[base] offset:-2048 sc1\n\t"
      "global_load_dwordx4 v[140:143], %[off], %[base] offset:-1024 sc1\n\t"
      "global_load_dwordx4 v[144:147], %[off], %[base] offset:0 sc1\n\t"
      "global_load_dwordx4 v[148:151], %[off], %[base] offset:1024 sc1\n\t"
      "s_sleep " STAG2 "\n\t"
      "1:\n\t"
      "global_load_dwordx4 v[152:155], %[off], %[base] offset:-4096 sc1\n\t"
      "global_load_dwordx4 v[156:159], %[off], %[base] offset:-3072 sc1\n\t"
      "global_load_dwordx4 v[160:163], %[off], %[base] offset:-2048 sc1\n\t"
      "global_load_dwordx4 v[164:167], %[off], %[base] offset:-1024 sc1\n\t"
      "global_load_dwordx4 v[168:171], %[off], %[base] offset:0 sc1\n\t"
      "global_load_dwordx4 v[172:175], %[off], %[base] offset:1024 sc1\n\t"
      "s_waitcnt vmcnt(6)\n\t"
      "v_bitop3_b32 v176, v129, v131, %[ta] bitop3:0x7e\n\tv_bitop3_b32 v182, v128, v130, %[tb] bitop3:0x7e\n\t"
      "v_bitop3_b32 v177, v133, v135, %[ta] bitop3:0x7e\n\tv_bitop3_b32 v183, v132, v134, %[tb] bitop3:0x7e\n\t"
      "v_bitop3_b32 v178, v137, v139, %[ta] bitop3:0x7e\n\tv_bitop3_b32 v184, v136, v138, %[tb] bitop3:0x7e\n\t"
      "v_bitop3_b32 v179, v141, v143, %[ta] bitop3:0x7e\n\tv_bitop3_b32 v185, v140, v142, %[tb] bitop3:0x7e\n\t"
      "v_bitop3_b32 v180, v145, v147, %[ta] bitop3:0x7e\n\tv_bitop3_b32 v186, v144, v146, %[tb] bitop3:0x7e\n\t"
      "v_bitop3_b32 v181, v149, v151, %[ta] bitop3:0x7e\n\tv_bitop3_b32 v187, v148, v150, %[tb] bitop3:0x7e\n\t"
      "v_or3_b32 v176, v176, v177, v178\n\tv_or3_b32 v182, v182, v183, v184\n\tv_or3_b32 v179, v179, v180, v181\n\tv_or3_b32 v182, v182, v185, v186\n\t"
      "v_or_b32 v182, v182, v187\n\tv_or_b32 v176, v176, v179\n\tv_and_or_b32 v176, v182, %[ff], v176\n\tv_cmp_ne_u32 vcc, 0, v176\n\t"
      "s_cbranch_vccz 2f\n\t"
      "global_load_dwordx4 v[128:131], %[off], %[base] offset:-4096 sc1\n\t"
      "global_load_dwordx4 v[132:135], %[off], %[base] offset:-3072 sc1\n\t"
      "global_load_dwordx4 v[136:139], %[off], %[base] offset:-2048 sc1\n\t"
      "global_load_dwordx4 v[140:143], %[off], %[base] offset:-1024 sc1\n\t"
      "global_load_dwordx4 v[144:147], %[off], %[base] offset:0 sc1\n\t"
      "global_load_dwordx4 v[148:151], %[off], %[base] offset:1024 sc1\n\t"
      "s_waitcnt vmcnt(6)\n\t"
      "v_bitop3_b32 v176, v153, v155, %[ta] bitop3:0x7e\n\tv_bitop3_b32 v182, v152, v154, %[tb] bitop3:0x7e\n\t"
      "v_bitop3_b32 v177, v157, v159, %[ta] bitop3:0x7e\n\tv_bitop3_b32 v183, v156, v158, %[tb] bitop3:0x7e\n\t"
      "v_bitop3_b32 v178, v161, v163, %[ta] bitop3:0x7e\n\tv_bitop3_b32 v184, v160, v162, %[tb] bitop3:0x7e\n\t"
      "v_bitop3_b32 v179, v165, v167, %[ta] bitop3:0x7e\n\tv_bitop3_b32 v185, v164, v166, %[tb] bitop3:0x7e\n\t"
      "v_bitop3_b32 v180, v169, v171, %[ta] bitop3:0x7e\n\tv_bitop3_b32 v186, v168, v170, %[tb] bitop3:0x7e\n\t"
      "v_bitop3_b32 v181, v173, v175, %[ta] bitop3:0x7e\n\tv_bitop3_b32 v187, v172, v174, %[tb] bitop3:0x7e\n\t"
      "v_or3_b32 v176, v176, v177, v178\n\tv_or3_b32 v182, v182, v183, v184\n\tv_or3_b32 v179, v179, v180, v181\n\tv_or3_b32 v182, v182, v185, v186\n\t"
      "v_or_b32 v182, v182, v187\n\tv_or_b32 v176, v176, v179\n\tv_and_or_b32 v176, v182, %[ff], v176\n\tv_cmp_ne_u32 vcc, 0, v176\n\t"
      "s_cbranch_vccz 3f\n\t"
      "s_sub_u32 %[n], %[n], 1\n\ts_cbranch_scc0 1b\n\t"
      "3:\n\t"
      "v_and_b32 %[a0], %[mk], v152\n\tv_and_b32 %[b0], %[mk], v154\n\t"
      "v_and_b32 %[a1], %[mk], v156\n\tv_and_b32 %[b1], %[mk], v158\n\t"
      "v_and_b32 %[a2], %[mk], v160\n\tv_and_b32 %[b2], %[mk], v162\n\t"
      "v_and_b32 %[a3], %[mk], v164\n\tv_and_b32 %[b3], %[mk], v166\n\t"
      "v_and_b32 %[a4], %[mk], v168\n\tv_and_b32 %[b4], %[mk], v170\n\t"
      "v_and_b32 %[a5], %[mk], v172\n\tv_and_b32 %[b5], %[mk], v174\n\t"
      "s_branch 5f\n\t"
      "2:\n\t"
      "v_and_b32 %[a0], %[mk], v128\n\tv_and_b32 %[b0], %[mk], v130\n\t"
      "v_and_b32 %[a1], %[mk], v132\n\tv_and_b32 %[b1], %[mk], v134\n\t"
      "v_and_b32 %[a2], %[mk], v136\n\tv_and_b32 %[b2], %[mk], v138\n\t"
      "v_and_b32 %[a3], %[mk], v140\n\tv_and_b32 %[b3], %[mk], v142\n\t"
      "v_and_b32 %[a4], %[mk], v144\n\tv_and_b32 %[b4], %[mk], v146\n\t"
      "v_and_b32 %[a5], %[mk], v148\n\tv_and_b32 %[b5], %[mk], v150\n\t"
      "5:\n\ts_nop 1"
      : [a0] "=&v"(sa[0]), [b0] "=&v"(sb[0]), [a1] "=&v"(sa[1]), [b1] "=&v"(sb[1]), [a2] "=&v"(sa[2]), [b2] "=&v"(sb[2]), [a3] "=&v"(sa[3]), [b3] "=&v"(sb[3]), [a4] "=&v"(sa[4]), [b4] "=&v"(sb[4]), [a5] "=&v"(sa[5]), [b5] "=&v"(sb[5]), [n] "+s"(spins)
      : [off] "v"(foff), [base] "s"(ws), [ta] "s"(tagA), [tb] "s"(tagB), [ff] "v"(vff), [mk] "s"(0xffffff00u)
      : "memory", "vcc", "scc", "v128", "v129", "v130", "v131", "v132", "v133", "v134", "v135", "v136", "v137", "v138", "v139", "v140", "v141", "v142", "v143", "v144", "v145", "v146", "v147", "v148", "v149", "v150", "v151", "v152", "v153", "v154", "v155", "v156", "v157", "v158", "v159", "v160", "v161", "v162", "v163", "v164", "v165", "v166", "v167", "v168", "v169", "v170", "v171", "v172", "v173", "v174", "v175", "v176", "v177", "v178", "v179", "v180", "v181", "v182", "v183", "v184", "v185", "v186", "v187");
    STAMP(10);
    SETX(14, SPIN_CAP - spins);
    const float S0 = sum_hi3(((sa[0] + sa[1]) + (sa[2] + sa[3])) + (sa[4] + sa[5])), S1 = sum_hi3(((sb[0] + sb[1]) + (sb[2] + sb[3])) + (sb[4] + sb[5]));
    const float y0 = x0 + bfr(bfr(S0) * ga), y1 = x1 + bfr(bfr(S1) * gb);
    if (lane < 8) *op = cvtpk(y0, y1);
    if (fl == 0 && lane == 0) __hip_atomic_store((GAS u64*)(ws + WS_EPOCH), ((u64)tagH << 32) | tagA, __ATOMIC_RELAXED, __HIP_MEMORY_SCOPE_AGENT);
    STAMP(11);
  }
  STAMP_FLUSH();
}

extern "C" void kernel_launch(void* const* d_in, const int* in_sizes, int n_in, void* d_out, int out_size, void* d_ws, size_t ws_size,
                              hipStream_t stream) {
  (void)in_sizes; (void)n_in; (void)out_size; (void)ws_size;
  fused_decode_kernel<<<NWG, NTHR, 0, stream>>>((const u16*)d_in[0], (const u16*)d_in[1], (const u16*)d_in[2], (const u16*)d_in[3],
                                                (const u16*)d_in[4], (const u16*)d_in[6], (const u16*)d_in[7], (const u16*)d_in[8],
                                                (const u16*)d_in[9], (u16*)d_out, (unsigned char*)d_ws);
}
```

```cpp
#define TLBW
#include <hip/hip_runtime.h>
#include <hip/hip_bf16.h>
#include <stdint.h>

#define GAS __attribute__((address_space(1)))
#define LAS __attribute__((address_space(3)))
#define LP(T, a) ((LAS T*)(size_t)(a))
typedef unsigned short u16;
typedef unsigned long long u64;
typedef unsigned v4u __attribute__((ext_vector_type(4)));
typedef float f32x4 __attribute__((ext_vector_type(4)));
typedef float f32x2 __attribute__((ext_vector_type(2)));
typedef short bf16x8 __attribute__((ext_vector_type(8)));
typedef __bf16 bf16x2 __attribute__((ext_vector_type(2)));

constexpr int DM = 640, NH = 32, DH = 192, SK = 32768, WIN = 4096, FF = 1536;
constexpr int NWG = 256, NTHR = 512;
constexpr int QN = NH * DH;

constexpr int WS_EPOCH = 0;
constexpr int WS_S1 = 4096;
constexpr int WS_ML = WS_S1 + 240 * 64 * 8;
constexpr int WS_OP = WS_ML + 256 * 128;
constexpr int FIN_STRIDE = 72 * 128;
constexpr int WS_FIN = WS_OP + 256 * 192 * 8;
constexpr int WS_END = WS_FIN + 40 * FIN_STRIDE;
#define STAMP(i) do { } while (0)
#define STAMP0() do { } while (0)
#define STAMP_DECL do { } while (0)
#define SETX(i, v) do { } while (0)
#define STAMP_FLUSH() do { } while (0)

constexpr int AUX_SC1 = 16;
#define DEPTH 3
#define WO_LAST
#define SLEEP1 "1"
#define STAG2 "5"
#define TSLEEP 1
#define TAIL_SLEEP() do { if (TSLEEP) __builtin_amdgcn_s_sleep(TSLEEP); } while (0)
constexpr unsigned SPIN_CAP = 1u << 18;

#define SB() __builtin_amdgcn_sched_barrier(0)
#define CFENCE() asm volatile("" ::: "memory")
#define WG_BARRIER() do { asm volatile("s_waitcnt lgkmcnt(0)" ::: "memory"); __builtin_amdgcn_s_barrier(); asm volatile("" ::: "memory"); } while (0)

#define AUX_STREAM 2
typedef __amdgpu_buffer_rsrc_t rsrc_t;
__device__ __forceinline__ rsrc_t mkrs(const void* p, unsigned bytes) { return __builtin_amdgcn_make_buffer_rsrc((void*)p, 0, bytes, 0x00020000); }
__device__ __forceinline__ v4u ldst(rsrc_t rs, unsigned voff, unsigned soff) { return __builtin_amdgcn_raw_buffer_load_b128(rs, voff, soff, AUX_STREAM); }
#define POLL_LD(dst, voff, base, imm) asm volatile("global_load_dwordx4 %0, %1, %2 offset:" #imm " sc1" : "=v"(dst) : "v"(voff), "s"(base) : "memory")
__device__ __forceinline__ float dot2(unsigned k, unsigned q, float acc) {
  return __builtin_amdgcn_fdot2_f32_bf16(__builtin_bit_cast(bf16x2, k), __builtin_bit_cast(bf16x2, q), acc, false);
}
__device__ __forceinline__ unsigned cvtpk(float lo, float hi) {
  bf16x2 v = {(__bf16)lo, (__bf16)hi};
  return __builtin_bit_cast(unsigned, v);
}
__device__ __forceinline__ float asf(unsigned u) { return __builtin_bit_cast(float, u); }
__device__ __forceinline__ unsigned asu(float f) { return __builtin_bit_cast(unsigned, f); }
__device__ __forceinline__ float bfr(float f) { return asf(cvtpk(f, f) << 16); }
__device__ __forceinline__ float bflo(unsigned u) { return asf(u << 16); }
__device__ __forceinline__ float bfhi(unsigned u) { return asf(u & 0xffff0000u); }
template <int CTRL> __device__ __forceinline__ float dpp(float x) {
  return __builtin_bit_cast(float, __builtin_amdgcn_mov_dpp(__builtin_bit_cast(int, x), CTRL, 0xf, 0xf, true));
}
constexpr int XOR1 = 0xB1, XOR2 = 0x4E, HMIR = 0x141, ROR8 = 0x128, ROR4 = 0x124;
__device__ __forceinline__ float sum8(float x) { x += dpp<XOR1>(x); x += dpp<XOR2>(x); x += dpp<HMIR>(x); return x; }
__device__ __forceinline__ float max8(float x) { x = fmaxf(x, dpp<XOR1>(x)); x = fmaxf(x, dpp<XOR2>(x)); x = fmaxf(x, dpp<HMIR>(x)); return x; }
__device__ __forceinline__ float sum_hi3(float x) {
  x += dpp<ROR8>(x);
  auto s = __builtin_amdgcn_permlane16_swap(asu(x), asu(x), false, false);
  x = asf(s[0]) + asf(s[1]);
  auto t = __builtin_amdgcn_permlane32_swap(asu(x), asu(x), false, false);
  return asf(t[0]) + asf(t[1]);
}
__device__ __forceinline__ float max_hi3(float x) {
  x = fmaxf(x, dpp<ROR8>(x));
  auto s = __builtin_amdgcn_permlane16_swap(asu(x), asu(x), false, false);
  x = fmaxf(asf(s[0]), asf(s[1]));
  auto t = __builtin_amdgcn_permlane32_swap(asu(x), asu(x), false, false);
  return fmaxf(asf(t[0]), asf(t[1]));
}
__device__ __forceinline__ f32x4 mfma(v4u a, v4u b, f32x4 c) {
  return __builtin_amdgcn_mfma_f32_16x16x32_bf16(__builtin_bit_cast(bf16x8, a), __builtin_bit_cast(bf16x8, b), c, 0, 0, 0);
}
__device__ __forceinline__ v4u sel(unsigned pp, const v4u& m) { return v4u{pp & m[0], pp & m[1], pp & m[2], pp & m[3]}; }
template <class T> __device__ __forceinline__ unsigned ldsaddr(T* p) { return (unsigned)(size_t)(LAS T*)p; }
template <int ROW> __device__ __forceinline__ f32x2 xw_sum(unsigned p) {
  const f32x2 s = (*LP(const f32x2, p) + *LP(const f32x2, p + ROW * 4)) + (*LP(const f32x2, p + ROW * 8) + *LP(const f32x2, p + ROW * 12));
  auto a = __builtin_amdgcn_permlane16_swap(asu(s[0]), asu(s[0]), false, false);
  auto c = __builtin_amdgcn_permlane16_swap(asu(s[1]), asu(s[1]), false, false);
  const float u = asf(a[0]) + asf(a[1]), v = asf(c[0]) + asf(c[1]);
  auto d = __builtin_amdgcn_permlane32_swap(asu(u), asu(u), false, false);
  auto e = __builtin_amdgcn_permlane32_swap(asu(v), asu(v), false, false);
  return f32x2{asf(d[0]) + asf(d[1]), asf(e[0]) + asf(e[1])};
}
__device__ __forceinline__ v4u gran2(float v0, float v1, unsigned tagA, unsigned tagB) {
  return v4u{(asu(v0) & 0xffffff00u) | tagB, tagA, (asu(v1) & 0xffffff00u) | tagB, tagA};
}
__device__ __forceinline__ unsigned gbad(const v4u& g, unsigned tagA, unsigned tagB) {
  return (g[1] ^ tagA) | (g[3] ^ tagA) | (((g[0] ^ tagB) | (g[2] ^ tagB)) & 0xffu);
}
__device__ __forceinline__ bool any_bad(unsigned bad) { return __builtin_amdgcn_ballot_w64(bad != 0u) != 0ull; }
__device__ __forceinline__ float gval(unsigned u) { return asf(u & 0xffffff00u); }

constexpr int I_W2 = 5, I_K = 6, I_V = 30, I_L = 54, I_END = 58;
#define D_INIT DEPTH
#define D_BAR DEPTH
#define D_END 4
constexpr int depth_at(int i) { return i < 0 ? D_INIT : (i == 4 || i == 17 || i == I_V) ? D_BAR : i >= I_END - 5 - 1 ? D_END : DEPTH; }
constexpr int upto(int i) { int u = -1; for (int k = -1; k <= i; ++k) { const int t = k + depth_at(k) < I_END - 1 ? k + depth_at(k) : I_END - 1; u = t > u ? t : u; } return u; }
constexpr int RA = 200, RB = 136, RS = 72;
struct alignas(16) Lds {
  float redA[16 * RA];
  float redB[16 * RB];
  float red1[16 * RS];
  float red2[16 * RS];
  unsigned qh[128];
  float wm[8];
  float wl[8];
  u16 xs[640];
};

__global__ void __launch_bounds__(NTHR, 2)
fused_decode_kernel(const u16*, const u16*, const u16*, const u16*, const u16*, const u16*, const u16*, const u16*, const u16*, u16*, unsigned char*) {
  typedef unsigned v16u __attribute__((ext_vector_type(16)));
  v16u ka; v4u kb; u64 kw;
  int b = blockIdx.x;
  asm volatile("s_load_dwordx16 %0, %4, 0x0\n\ts_load_dwordx4 %1, %4, 0x40\n\ts_load_dwordx2 %2, %4, 0x50"
               : "=&s"(ka), "=&s"(kb), "=&s"(kw), "+s"(b) : "s"(__builtin_amdgcn_kernarg_segment_ptr()));
  SB();
  __shared__ Lds L;
  STAMP_DECL;
  STAMP0();
  asm volatile("" ::: "v187");
  const int tid = threadIdx.x, lane = tid & 63, w = __builtin_amdgcn_readfirstlane(tid >> 6), r = lane >> 3, c = lane & 7;
  const int h = (b & 7) * 4 + ((b >> 3) & 3), j = b >> 5;
  const bool isq = j < 6;
  const int u1 = (j == 6) ? h : 32 + h;
  const bool has1 = isq || u1 < 48;
  const int mq = (j - 6) >> 31;
  int cg = ((3 * h + (j >> 1)) & mq) | ((96 + (u1 >> 1)) & ~mq), half = ((j & mq) | (u1 & ~mq)) & 1;
  unsigned stride = FF * 2 + ((QN * 2 - FF * 2) & mq);
  unsigned o1 = (cg - (96 & ~mq)) * 128, n1 = has1 ? DM * stride : 0u;
  unsigned off1 = (half * 320 + w * 8 + r) * stride + c * 16;
  unsigned kvrow = h * SK + (SK - WIN) + (w * 8 + j) * 8;
  constexpr int BSTR = 512 * DH * 2;
  u64 kvoff = (u64)kvrow * (DH * 2);
  unsigned loff = (unsigned)(r * (DH * 2) + c * 16);
  const int vu = b < 240 ? b : 239;
  int rg = vu / 10, cgrp = vu - rg * 10;
  unsigned o2 = (rg * 64 + w * 8) * (DM * 2) + cgrp * 128;
  const int P = h >> 1, jj = (h & 1) * 8 + j;
  int rgp = jj / 5, cg5 = jj - rgp * 5;
  const bool isred = jj == 15;
  const int rowb = 128 * rgp + 16 * w;
  unsigned ol = (P * 384 + rowb) * (DM * 2) + cg5 * 256;
  unsigned offl = (lane >> 4) * (4 * DM * 2) + (lane & 15) * 16, off2 = r * (DM * 2) + c * 16, offx = lane * 16, offx2 = (lane & 15) * 16;
  asm volatile("" : "+s"(cg), "+s"(half), "+s"(stride), "+s"(o1), "+s"(n1), "+s"(kvoff), "+s"(rg), "+s"(cgrp), "+s"(o2), "+s"(rgp), "+s"(cg5), "+s"(ol));
  asm volatile("" : "+v"(off1), "+v"(loff), "+v"(offl), "+v"(off2), "+v"(offx), "+v"(offx2));
  SB();
  asm volatile("s_waitcnt lgkmcnt(0)" : "+s"(ka), "+s"(kb), "+s"(kw));
#define KARG(v, i) ((const unsigned char*)(((u64)(v)[2 * (i) + 1] << 32) | (v)[2 * (i)]))
  const unsigned char *x_ = KARG(ka, 0), *kc_ = KARG(ka, 1), *vc_ = KARG(ka, 2), *wq_ = KARG(ka, 3), *wo_ = KARG(ka, 4), *w1_ = KARG(ka, 5), *b1_ = KARG(ka, 6),
                      *w2_ = KARG(ka, 7), *b2_ = KARG(kb, 0), *out_ = KARG(kb, 1), *ws_ = (const unsigned char*)kw;
  GAS unsigned char* ws = (GAS unsigned char*)ws_;
  const GAS unsigned char* x = (const GAS unsigned char*)x_;
  const rsrc_t rw1 = mkrs((const unsigned char*)(isq ? wq_ : w1_) + o1, n1);
  rsrc_t RW2, RWO, RK, RV;
  v4u R[I_END];
#define ISSUE(i) do { if constexpr ((i) < I_W2) R[i] = ldst(rw1, off1, (i) * 64 * stride); \
    else if constexpr ((i) == I_W2) R[i] = ldst(RW2, off2, 0); \
    else if constexpr ((i) >= I_K && (i) < I_K + 24) R[i] = ldst(RK, loff + (((i) - I_K) % 3) * 128, (((i) - I_K) / 3) * BSTR); \
    else if constexpr ((i) >= I_V && (i) < I_V + 24) R[i] = ldst(RV, loff + (((i) - I_V) % 3) * 128, (((i) - I_V) / 3) * BSTR); \
    else if constexpr ((i) >= I_L && (i) < I_L + 4) R[i] = ldst(RWO, offl + ((i) - I_L) * (DM * 2), 0); \
    SB(); } while (0)
#define TOUCH(i) do { asm volatile("" : "+v"(R[i])); SB(); } while (0)
#define STEP(i) do { constexpr int u0_ = upto((i) - 1), u1_ = upto(i); TOUCH(i); if constexpr (u0_ < u1_) ISSUE(u0_ + 1); if constexpr (u0_ + 1 < u1_) ISSUE(u0_ + 2); \
    if constexpr (u0_ + 2 < u1_) ISSUE(u0_ + 3); } while (0)
#define STEP3(i) do { STEP(i); STEP((i) + 1); STEP((i) + 2); } while (0)

  const v4u xa = *(const GAS v4u*)(x + offx);
  const v4u xb = *(const GAS v4u*)(x + 1024 + offx2);
  SB();
  ISSUE(0); ISSUE(1);
  if constexpr (upto(-1) >= 2) ISSUE(2);
  if constexpr (upto(-1) >= 3) ISSUE(3);
  static_assert(upto(-1) <= 3 && upto(I_V + 23) == I_END - 1, "issue schedule");
  u64 ep;
  asm volatile("s_load_dwordx2 %0, %1, 0x0" : "=s"(ep) : "s"(ws_) : "memory");
  SB();
  STAMP(1);
  SETX(15, (unsigned)__builtin_amdgcn_s_getreg(20 | (3 << 11)) & 0xfu);
  const rsrc_t rs = mkrs(ws_, WS_END);
  RW2 = mkrs((const unsigned char*)w2_ + o2, b < 240 ? 8 * DM * 2 : 0);
  RWO = mkrs((const unsigned char*)wo_ + ol, isred ? 0 : 16 * DM * 2);
  RK = mkrs((const unsigned char*)kc_ + kvoff, 7 * BSTR + 8 * DH * 2);
  RV = mkrs((const unsigned char*)vc_ + kvoff, 7 * BSTR + 8 * DH * 2);

  v4u msk;
#pragma unroll
  for (int d = 0; d < 4; ++d) msk[d] = ((c >> 1) == d) ? ((c & 1) ? 0xffff0000u : 0x0000ffffu) : 0u;

  STEP(0); STEP(1);
  if constexpr (D_INIT >= 3) STEP(2);
  if constexpr (D_INIT >= 4) STEP(3);
  float mean_b, rden;
  unsigned xr[5];
  {
    const bool hb = lane < 16;
    *(v4u*)&L.xs[lane * 8] = xa;
    if (hb) *(v4u*)&L.xs[512 + lane * 8] = xb;
#pragma unroll
    for (int p = 0; p < 5; ++p) xr[p] = L.xs[half * 320 + p * 64 + w * 8 + r];
    float e[16];
#pragma unroll
    for (int k = 0; k < 4; ++k) { e[2 * k] = bflo(xa[k]); e[2 * k + 1] = bfhi(xa[k]); e[8 + 2 * k] = bflo(xb[k]); e[9 + 2 * k] = bfhi(xb[k]); }
    float s = 0.f, s2 = 0.f;
#pragma unroll
    for (int k = 0; k < 8; ++k) { s += e[k]; s2 += e[8 + k]; }
    s += hb ? s2 : 0.f;
    s = sum_hi3(sum8(s));
    const float mean = s * (1.f / DM);
    float v = 0.f, v2 = 0.f;
#pragma unroll
    for (int k = 0; k < 8; ++k) { const float d0 = e[k] - mean, d1 = e[8 + k] - mean; v = fmaf(d0, d0, v); v2 = fmaf(d1, d1, v2); }
    v += hb ? v2 : 0.f;
    v = sum_hi3(sum8(v));
    mean_b = bfr(mean);
    rden = __builtin_amdgcn_rcpf(bfr(__builtin_amdgcn_sqrtf(bfr(bfr(v * (1.f / DM)) + 1e-5f))));
  }
  SB();
#define PRE_STEPS() do { } while (0)
#define MID_STEPS() do { if constexpr (D_INIT < 3) STEP(2); if constexpr (D_INIT < 4) STEP(3); } while (0)
  const bool dvalid = ((lane >> 3) & 1) == (lane >> 5);
  const int drow = w * 2 + (lane >> 5), dpos = c * 8 + ((lane >> 4) & 1) * 4;
  const int l16 = lane & 15;
  {
    v4u a1[5];
#pragma unroll
    for (int p = 0; p < 5; ++p) { const float xn = bfr(bflo(xr[p]) - mean_b) * rden; a1[p] = sel(cvtpk(xn, xn), msk); }
    SB();
    MID_STEPS(); STEP(4);
    f32x4 acc = {0.f, 0.f, 0.f, 0.f};
#pragma unroll
    for (int p = 0; p < 5; ++p) acc = mfma(a1[p], R[p], acc);
    if (dvalid) *(f32x4*)&L.red1[drow * RS + dpos] = acc;
  }
  WG_BARRIER();
  asm volatile("" : "+s"(ep));
  const u64 tg = ep + 1;
  const unsigned tagA = (unsigned)tg, tagH = (unsigned)(tg >> 32), tagB = tagH & 0xffu;
  if (w >= 6 && has1) {
    const int pr = (w - 6) * 16 + l16;
    const f32x2 s = xw_sum<RS>(ldsaddr(L.red1) + ((lane >> 4) * (4 * RS) + 2 * pr) * 4);
    if (lane < 16) __builtin_amdgcn_raw_buffer_store_b128(gran2(s[0], s[1], tagA, tagB), rs, WS_S1 + (cg * 2 + half) * 512 + pr * 16, 0, AUX_SC1);
  }
  unsigned tw0 = 0, tw1 = 0;
  if (w >= 2 && w < 6) {
    const unsigned char* p0 = w == 2 ? vc_ + kvoff : w == 3 ? wo_ + (isred ? 0u : ol) : w == 4 ? ws_ + WS_OP : out_;
    const unsigned char* p1 = w == 2 ? vc_ + kvoff + 7 * BSTR : w == 3 ? wo_ + (isred ? 0u : ol + 15 * DM * 2) : w == 4 ? ws_ + WS_END - 4 : b2_;
    asm volatile("s_load_dword %0, %2, 0x0\n\ts_load_dword %1, %3, 0x0" : "=&s"(tw0), "=&s"(tw1) : "s"(p0), "s"(p1) : "memory");
  }
  SB();
  STAMP(2);
  STEP(5); STEP3(6); STEP3(9); STEP3(12);
  v4u g0, g1; unsigned b1p;
  const unsigned goff = WS_S1 + (tid < 96 ? (3 * h + (tid >> 5)) * 1024 + (tid & 31) * 16 : (96 + rg) * 1024 + (tid & 31) * 16);
  if (w < 2) {
    POLL_LD(g0, goff, ws, 0); POLL_LD(g1, goff, ws, 512);
    asm volatile("global_load_dword %0, %1, %2" : "=v"(b1p) : "v"(rg * 128 + (lane & 31) * 4), "s"(b1_) : "memory");
  }
  SB();
  STEP3(15);
  if (w < 2) {
    asm volatile("s_waitcnt vmcnt(%3)" : "+v"(g0), "+v"(g1), "+v"(b1p) : "n"(upto(17) - upto(14)) : "memory");
    unsigned spins = 0;
    while (any_bad(gbad(g0, tagA, tagB) | gbad(g1, tagA, tagB))) {
      if (++spins > SPIN_CAP) break;
      __builtin_amdgcn_s_sleep(1);
      asm volatile("global_load_dwordx4 %0, %2, %3 sc1\n\tglobal_load_dwordx4 %1, %2, %3 offset:512 sc1\n\ts_waitcnt vmcnt(0)"
                   : "=&v"(g0), "=&v"(g1) : "v"(goff), "s"(ws) : "memory");
    }
    SETX(12, spins);
    float a0 = gval(g0[0]) + gval(g1[0]), a1 = gval(g0[2]) + gval(g1[2]);
    if (tid >= 96) {
      a0 = fmaxf(bfr(bfr(a0) + bflo(b1p)), 0.f); a1 = fmaxf(bfr(bfr(a1) + bfhi(b1p)), 0.f);
      a0 *= a0; a1 *= a1;
    }
    L.qh[tid] = cvtpk(a0, a1);
  }
  WG_BARRIER();
  asm volatile("" :: "s"(tw0), "s"(tw1));
  STAMP(5);
  float sc[8];
  {
    v4u q[3];
#pragma unroll
    for (int i = 0; i < 3; ++i) q[i] = *(const v4u*)&L.qh[i * 32 + c * 4];
    const unsigned hv = ((const u16*)&L.qh[96])[w * 8 + r];
    constexpr float C2 = 0.07216878364870322f * 1.4426950408889634f;
#define QK(bk) do { float d_ = 0.f; _Pragma("unroll") for (int i = 0; i < 3; ++i) _Pragma("unroll") for (int k = 0; k < 4; ++k) d_ = dot2(R[I_K + 3 * (bk) + i][k], q[i][k], d_); \
    sc[bk] = sum8(d_) * C2; SB(); } while (0)
#define W2MM() do { const f32x4 acc_ = mfma(sel(hv | (hv << 16), msk), R[I_W2], f32x4{0.f, 0.f, 0.f, 0.f}); if (dvalid) *(f32x4*)&L.red2[drow * RS + dpos] = acc_; SB(); } while (0)
    STEP(18); QK(0); STEP(19); QK(1); STEP(20); QK(2); STEP(21); QK(3); STEP(22); QK(4); STEP(23); QK(5);
    STEP(24); W2MM(); STEP(25); STEP(26); QK(6); STEP3(27); QK(7);
  }
  {
    float m = sc[0];
#pragma unroll
    for (int bk = 1; bk < 8; ++bk) m = fmaxf(m, sc[bk]);
    m = max_hi3(m);
    float l = 0.f;
#pragma unroll
    for (int bk = 0; bk < 8; ++bk) l += __builtin_amdgcn_exp2f(sc[bk] - m);
    l = sum_hi3(l);
    if (lane == 0) { L.wm[w] = m; L.wl[w] = l; }
  }
  SB();
  STEP(I_V);
  WG_BARRIER();
  const float mw = L.wm[c], M = max8(mw);
  if (w == 5) {
    const float l = sum8(__builtin_amdgcn_exp2f(mw - M) * L.wl[c]);
    if (lane < 8) __builtin_amdgcn_raw_buffer_store_b128(v4u{asu(M), tagA, (asu(l) & 0xffffff00u) | tagB, tagA}, rs, WS_ML + (h * 8 + j) * 128 + lane * 16, 0, AUX_SC1);
  }
  SB();
  v4u A[8];
#pragma unroll
  for (int bk = 0; bk < 8; ++bk) { const float p = __builtin_amdgcn_exp2f(sc[bk] - M); A[bk] = sel(cvtpk(p, p), msk); }
  SB();
  STEP(I_V + 1);
  if (w >= 6 && b < 240) {
    const int pr = (w - 6) * 16 + l16;
    const f32x2 s = xw_sum<RS>(ldsaddr(L.red2) + ((lane >> 4) * (4 * RS) + 2 * pr) * 4);
    if (lane < 16) __builtin_amdgcn_raw_buffer_store_b128(gran2(s[0], s[1], tagA, tagB), rs, WS_FIN + (cgrp * 4 + (pr >> 3)) * FIN_STRIDE + (48 + rg) * 128 + (pr & 7) * 16, 0, AUX_SC1);
  }
  SB();
  STAMP(6);
  float F;
  const int prw = w * 16 + l16;
  unsigned aA = ldsaddr(L.redA) + ((lane >> 4) * (4 * RA) + 2 * prw) * 4, aAw = ldsaddr(L.redA) + (drow * RA + dpos) * 4;
  unsigned aB = ldsaddr(L.redB) + ((lane >> 4) * (4 * RB) + 2 * prw) * 4, aBw = ldsaddr(L.redB) + (drow * RB + l16 * 8 + ((lane >> 4) & 1) * 4) * 4;
  unsigned sA = WS_OP + (h * 8 + j) * 1536 + prw * 16;
  unsigned sB = WS_FIN + (cg5 * 8 + (prw >> 3)) * FIN_STRIDE + (P * 3 + rgp) * 128 + (prw & 7) * 16;
  unsigned ooff = WS_OP + ((2 * P + (rowb >= DH ? 1 : 0)) * 8 + c) * 1536 + (rowb >= DH ? rowb - DH : rowb) * 8 + r * 16;
  unsigned vff = 0xffu;
  const v4u z4 = {0u, 0u, 0u, 0u};
  v4u mk0 = ((lane >> 3) & 1) ? z4 : msk, mk1 = ((lane >> 3) & 1) ? msk : z4;
  {
    v4u gm;
    const unsigned moff = WS_ML + (h * 8 + c) * 128 + r * 16;
    constexpr int NAFTER = upto(I_V + 20) - upto(I_V + 17);
    f32x4 acc[3] = {{0.f, 0.f, 0.f, 0.f}, {0.f, 0.f, 0.f, 0.f}, {0.f, 0.f, 0.f, 0.f}};
#define PV(i) do { acc[(i) % 3] = mfma(A[(i) / 3], R[I_V + (i)], acc[(i) % 3]); SB(); } while (0)
#define SPV(i) do { STEP(I_V + (i)); PV(i); } while (0)
#define SPV3(i) do { SPV(i); SPV((i) + 1); SPV((i) + 2); } while (0)
    PV(0); PV(1); SPV(2); SPV3(3); SPV3(6); SPV3(9); SPV3(12); SPV3(15);
    POLL_LD(gm, moff, ws, 0);
    SB();
    SPV3(18);
    asm volatile("s_waitcnt vmcnt(%1)" : "+v"(gm) : "n"(NAFTER) : "memory");
    {
      unsigned spins = 0;
      while (any_bad((gm[1] ^ tagA) | (gm[3] ^ tagA) | ((gm[2] ^ tagB) & 0xffu))) {
        if (++spins > SPIN_CAP) break;
        __builtin_amdgcn_s_sleep(2);
        asm volatile("global_load_dwordx4 %0, %1, %2 sc1\n\ts_waitcnt vmcnt(0)" : "=&v"(gm) : "v"(moff), "s"(ws) : "memory");
      }
      const float mc = asf(gm[0]), Mh = max8(mc);
      F = __builtin_amdgcn_exp2f(M - Mh) * __builtin_amdgcn_rcpf(sum8(__builtin_amdgcn_exp2f(mc - Mh) * gval(gm[2])));
    }
    asm volatile("" : "+v"(F), "+v"(aA), "+v"(aAw), "+v"(sA), "+v"(ooff), "+v"(aB), "+v"(aBw), "+v"(sB), "+v"(mk0), "+v"(mk1), "+v"(vff));
    SB();
    SPV3(21);
    if (dvalid) {
#pragma unroll
      for (int i = 0; i < 3; ++i) *LP(f32x4, aAw + i * 256) = acc[i];
    }
  }
  STAMP(7);
  SETX(14, __builtin_amdgcn_s_memtime() - cyc0);
  WG_BARRIER();
  STAMP(3);
  if (w < 6) {
    const f32x2 s = xw_sum<RA>(aA);
    if (lane < 16) __builtin_amdgcn_raw_buffer_store_b128(gran2(s[0] * F, s[1] * F, tagA, tagB), rs, sA, 0, AUX_SC1);
  }
  SB();
  STAMP(4);
  if (!isred) {
    float o0, o1;
    unsigned spins = SPIN_CAP;
    u64 sv;
    asm volatile(
      "s_mov_b64 %[sv], exec\n\t"
      "1:\n\t"
      "global_load_dwordx4 v[128:131], %[off], %[base] sc1\n\t"
      "s_waitcnt vmcnt(0)\n\t"
      "v_bitop3_b32 v184, v129, v131, %[ta] bitop3:0x7e\n\tv_bitop3_b32 v185, v128, v130, %[tb] bitop3:0x7e\n\t"
      "v_and_or_b32 v184, v185, %[ff], v184\n\tv_cmp_ne_u32 vcc, 0, v184\n\t"
      "s_and_b64 exec, exec, vcc\n\t"
      "s_cbranch_scc0 2f\n\t"
      "s_sleep " SLEEP1 "\n\t"
      "s_sub_u32 %[n], %[n], 1\n\ts_cbranch_scc0 1b\n\t"
      "2:\n\t"
      "s_mov_b64 exec, %[sv]\n\t"
      "v_and_b32 %[o0], %[mk], v128\n\tv_and_b32 %[o1], %[mk], v130\n\ts_nop 1"
      : [o0] "=&v"(o0), [o1] "=&v"(o1), [n] "+s"(spins), [sv] "=&s"(sv)
      : [off] "v"(ooff), [base] "s"(ws), [ta] "s"(tagA), [tb] "s"(tagB), [ff] "v"(vff), [mk] "s"(0xffffff00u)
      : "memory", "vcc", "scc", "v128", "v129", "v130", "v131", "v184", "v185");
    STAMP(8);
    SETX(13, SPIN_CAP - spins);
    o0 = sum8(o0); o1 = sum8(o1);
    const unsigned p0 = cvtpk(o0, o0), p1 = cvtpk(o1, o1);
    f32x4 acc = mfma(sel(p0, mk0), R[I_L], f32x4{0.f, 0.f, 0.f, 0.f});
    acc = mfma(sel(p1, mk0), R[I_L + 1], acc);
    acc = mfma(sel(p0, mk1), R[I_L + 2], acc);
    acc = mfma(sel(p1, mk1), R[I_L + 3], acc);
    *LP(f32x4, aBw) = acc;
    WG_BARRIER();
    if (w < 4) {
      const f32x2 s = xw_sum<RB>(aB);
      if (lane < 16) __builtin_amdgcn_raw_buffer_store_b128(gran2(s[0], s[1], tagA, tagB), rs, sB, 0, AUX_SC1);
    }
    STAMP(9);
  } else if (P + 16 * w < 40) {
    const int fl = P + 16 * w;
    const unsigned xp = *(const GAS unsigned*)(x + fl * 32 + c * 4), b2p = *(const GAS unsigned*)((const GAS unsigned char*)b2_ + fl * 32 + c * 4);
    const unsigned foff = WS_FIN + fl * FIN_STRIDE + 4096 + lane * 16;
    v4u a[6];
    unsigned spins = 0;
    for (;;) {
      asm volatile("global_load_dwordx4 %0, %3, %4 offset:2048 sc1\n\tglobal_load_dwordx4 %1, %3, %4 offset:3072 sc1\n\tglobal_load_dwordx4 %2, %5, %4 sc1\n\ts_waitcnt vmcnt(0)"
                   : "=&v"(a[0]), "=&v"(a[1]), "=&v"(a[2]) : "v"(foff), "s"(ws), "v"(foff + 4096) : "memory");
      const unsigned bad = gbad(a[0], tagA, tagB) | gbad(a[1], tagA, tagB) | gbad(a[2], tagA, tagB);
      if (!any_bad(bad) || ++spins > SPIN_CAP) break;
      __builtin_amdgcn_s_sleep(1);
    }
    const float O0 = sum_hi3((gval(a[0][0]) + gval(a[1][0])) + gval(a[2][0])), O1 = sum_hi3((gval(a[0][2]) + gval(a[1][2])) + gval(a[2][2]));
    const float t0 = bfr(bfr(O0) + bflo(b2p)), t1 = bfr(bfr(O1) + bfhi(b2p));
    constexpr float L2E = 1.4426950408889634f;
    float ga = bfr(t0 * __builtin_amdgcn_rcpf(1.f + __builtin_amdgcn_exp2f(-L2E * t0)));
    float gb = bfr(t1 * __builtin_amdgcn_rcpf(1.f + __builtin_amdgcn_exp2f(-L2E * t1)));
    GAS unsigned* op = (GAS unsigned*)((GAS unsigned char*)out_ + fl * 32 + c * 4);
    float x0 = bflo(xp), x1 = bfhi(xp);
    asm volatile("" : "+v"(ga), "+v"(gb), "+v"(op), "+v"(x0), "+v"(x1));
    STAMP(9);
    float sa[6], sb[6];
    spins = SPIN_CAP;
    asm volatile(
      "global_load_dwordx4 v[128:131], %[off], %[base] offset:-4096 sc1\n\t"
      "global_load_dwordx4 v[132:135], %[off], %[base] offset:-3072 sc1\n\t"
      "global_load_dwordx4 v[136:139], %[off], %[base] offset:-2048 sc1\n\t"
      "global_load_dwordx4 v[140:143], %[off], %[base] offset:-1024 sc1\n\t"
      "global_load_dwordx4 v[144:147], %[off], %[base] offset:0 sc1\n\t"
      "global_load_dwordx4 v[148:151], %[off], %[base] offset:1024 sc1\n\t"
      "s_sleep " STAG2 "\n\t"
      "1:\n\t"
      "global_load_dwordx4 v[152:155], %[off], %[base] offset:-4096 sc1\n\t"
      "global_load_dwordx4 v[156:159], %[off], %[base] offset:-3072 sc1\n\t"
      "global_load_dwordx4 v[160:163], %[off], %[base] offset:-2048 sc1\n\t"
      "global_load_dwordx4 v[164:167], %[off], %[base] offset:-1024 sc1\n\t"
      "global_load_dwordx4 v[168:171], %[off], %[base] offset:0 sc1\n\t"
      "global_load_dwordx4 v[172:175], %[off], %[base] offset:1024 sc1\n\t"
      "s_waitcnt vmcnt(6)\n\t"
      "v_bitop3_b32 v176, v129, v131, %[ta] bitop3:0x7e\n\tv_bitop3_b32 v182, v128, v130, %[tb] bitop3:0x7e\n\t"
      "v_bitop3_b32 v177, v133, v135, %[ta] bitop3:0x7e\n\tv_bitop3_b32 v183, v132, v134, %[tb] bitop3:0x7e\n\t"
      "v_bitop3_b32 v178, v137, v139, %[ta] bitop3:0x7e\n\tv_bitop3_b32 v184, v136, v138, %[tb] bitop3:0x7e\n\t"
      "v_bitop3_b32 v179, v141, v143, %[ta] bitop3:0x7e\n\tv_bitop3_b32 v185, v140, v142, %[tb] bitop3:0x7e\n\t"
      "v_bitop3_b32 v180, v145, v147, %[ta] bitop3:0x7e\n\tv_bitop3_b32 v186, v144, v146, %[tb] bitop3:0x7e\n\t"
      "v_bitop3_b32 v181, v149, v151, %[ta] bitop3:0x7e\n\tv_bitop3_b32 v187, v148, v150, %[tb] bitop3:0x7e\n\t"
      "v_or3_b32 v176, v176, v177, v178\n\tv_or3_b32 v182, v182, v183, v184\n\tv_or3_b32 v179, v179, v180, v181\n\tv_or3_b32 v182, v182, v185, v186\n\t"
      "v_or_b32 v182, v182, v187\n\tv_or_b32 v176, v176, v179\n\tv_and_or_b32 v176, v182, %[ff], v176\n\tv_cmp_ne_u32 vcc, 0, v176\n\t"
      "s_cbranch_vccz 2f\n\t"
      "global_load_dwordx4 v[128:131], %[off], %[base] offset:-4096 sc1\n\t"
      "global_load_dwordx4 v[132:135], %[off], %[base] offset:-3072 sc1\n\t"
      "global_load_dwordx4 v[136:139], %[off], %[base] offset:-2048 sc1\n\t"
      "global_load_dwordx4 v[140:143], %[off], %[base] offset:-1024 sc1\n\t"
      "global_load_dwordx4 v[144:147], %[off], %[base] offset:0 sc1\n\t"
      "global_load_dwordx4 v[148:151], %[off], %[base] offset:1024 sc1\n\t"
      "s_waitcnt vmcnt(6)\n\t"
      "v_bitop3_b32 v176, v153, v155, %[ta] bitop3:0x7e\n\tv_bitop3_b32 v182, v152, v154, %[tb] bitop3:0x7e\n\t"
      "v_bitop3_b32 v177, v157, v159, %[ta] bitop3:0x7e\n\tv_bitop3_b32 v183, v156, v158, %[tb] bitop3:0x7e\n\t"
      "v_bitop3_b32 v178, v161, v163, %[ta] bitop3:0x7e\n\tv_bitop3_b32 v184, v160, v162, %[tb] bitop3:0x7e\n\t"
      "v_bitop3_b32 v179, v165, v167, %[ta] bitop3:0x7e\n\tv_bitop3_b32 v185, v164, v166, %[tb] bitop3:0x7e\n\t"
      "v_bitop3_b32 v180, v169, v171, %[ta] bitop3:0x7e\n\tv_bitop3_b32 v186, v168, v170, %[tb] bitop3:0x7e\n\t"
      "v_bitop3_b32 v181, v173, v175, %[ta] bitop3:0x7e\n\tv_bitop3_b32 v187, v172, v174, %[tb] bitop3:0x7e\n\t"
      "v_or3_b32 v176, v176, v177, v178\n\tv_or3_b32 v182, v182, v183, v184\n\tv_or3_b32 v179, v179, v180, v181\n\tv_or3_b32 v182, v182, v185, v186\n\t"
      "v_or_b32 v182, v182, v187\n\tv_or_b32 v176, v176, v179\n\tv_and_or_b32 v176, v182, %[ff], v176\n\tv_cmp_ne_u32 vcc, 0, v176\n\t"
      "s_cbranch_vccz 3f\n\t"
      "s_sub_u32 %[n], %[n], 1\n\ts_cbranch_scc0 1b\n\t"
      "3:\n\t"
      "v_and_b32 %[a0], %[mk], v152\n\tv_and_b32 %[b0], %[mk], v154\n\t"
      "v_and_b32 %[a1], %[mk], v156\n\tv_and_b32 %[b1], %[mk], v158\n\t"
      "v_and_b32 %[a2], %[mk], v160\n\tv_and_b32 %[b2], %[mk], v162\n\t"
      "v_and_b32 %[a3], %[mk], v164\n\tv_and_b32 %[b3], %[mk], v166\n\t"
      "v_and_b32 %[a4], %[mk], v168\n\tv_and_b32 %[b4], %[mk], v170\n\t"
      "v_and_b32 %[a5], %[mk], v172\n\tv_and_b32 %[b5], %[mk], v174\n\t"
      "s_branch 5f\n\t"
      "2:\n\t"
      "v_and_b32 %[a0], %[mk], v128\n\tv_and_b32 %[b0], %[mk], v130\n\t"
      "v_and_b32 %[a1], %[mk], v132\n\tv_and_b32 %[b1], %[mk], v134\n\t"
      "v_and_b32 %[a2], %[mk], v136\n\tv_and_b32 %[b2], %[mk], v138\n\t"
      "v_and_b32 %[a3], %[mk], v140\n\tv_and_b32 %[b3], %[mk], v142\n\t"
      "v_and_b32 %[a4], %[mk], v144\n\tv_and_b32 %[b4], %[mk], v146\n\t"
      "v_and_b32 %[a5], %[mk], v148\n\tv_and_b32 %[b5], %[mk], v150\n\t"
      "5:\n\ts_nop 1"
      : [a0] "=&v"(sa[0]), [b0] "=&v"(sb[0]), [a1] "=&v"(sa[1]), [b1] "=&v"(sb[1]), [a2] "=&v"(sa[2]), [b2] "=&v"(sb[2]), [a3] "=&v"(sa[3]), [b3] "=&v"(sb[3]), [a4] "=&v"(sa[4]), [b4] "=&v"(sb[4]), [a5] "=&v"(sa[5]), [b5] "=&v"(sb[5]), [n] "+s"(spins)
      : [off] "v"(foff), [base] "s"(ws), [ta] "s"(tagA), [tb] "s"(tagB), [ff] "v"(vff), [mk] "s"(0xffffff00u)
      : "memory", "vcc", "scc", "v128", "v129", "v130", "v131", "v132", "v133", "v134", "v135", "v136", "v137", "v138", "v139", "v140", "v141", "v142", "v143", "v144", "v145", "v146", "v147", "v148", "v149", "v150", "v151", "v152", "v153", "v154", "v155", "v156", "v157", "v158", "v159", "v160", "v161", "v162", "v163", "v164", "v165", "v166", "v167", "v168", "v169", "v170", "v171", "v172", "v173", "v174", "v175", "v176", "v177", "v178", "v179", "v180", "v181", "v182", "v183", "v184", "v185", "v186", "v187");
    STAMP(10);
    SETX(14, SPIN_CAP - spins);
    const float S0 = sum_hi3(((sa[0] + sa[1]) + (sa[2] + sa[3])) + (sa[4] + sa[5])), S1 = sum_hi3(((sb[0] + sb[1]) + (sb[2] + sb[3])) + (sb[4] + sb[5]));
    const float y0 = x0 + bfr(bfr(S0) * ga), y1 = x1 + bfr(bfr(S1) * gb);
    if (lane < 8) *op = cvtpk(y0, y1);
    if (fl == 0 && lane == 0) __hip_atomic_store((GAS u64*)(ws + WS_EPOCH), ((u64)tagH << 32) | tagA, __ATOMIC_RELAXED, __HIP_MEMORY_SCOPE_AGENT);
    STAMP(11);
  }
  STAMP_FLUSH();
}

extern "C" void kernel_launch(void* const* d_in, const int* in_sizes, int n_in, void* d_out, int out_size, void* d_ws, size_t ws_size,
                              hipStream_t stream) {
  (void)in_sizes; (void)n_in; (void)out_size; (void)ws_size;
  fused_decode_kernel<<<NWG, NTHR, 0, stream>>>((const u16*)d_in[0], (const u16*)d_in[1], (const u16*)d_in[2], (const u16*)d_in[3],
                                                (const u16*)d_in[4], (const u16*)d_in[6], (const u16*)d_in[7], (const u16*)d_in[8],
                                                (const u16*)d_in[9], (u16*)d_out, (unsigned char*)d_ws);
}
```

```cpp
#define PAD_TO 2400
#include <hip/hip_runtime.h>
#include <hip/hip_bf16.h>
#include <stdint.h>

#define GAS __attribute__((address_space(1)))
#define LAS __attribute__((address_space(3)))
#define LP(T, a) ((LAS T*)(size_t)(a))
typedef unsigned short u16;
typedef unsigned long long u64;
typedef unsigned v4u __attribute__((ext_vector_type(4)));
typedef float f32x4 __attribute__((ext_vector_type(4)));
typedef float f32x2 __attribute__((ext_vector_type(2)));
typedef short bf16x8 __attribute__((ext_vector_type(8)));
typedef __bf16 bf16x2 __attribute__((ext_vector_type(2)));

constexpr int DM = 640, NH = 32, DH = 192, SK = 32768, WIN = 4096, FF = 1536;
constexpr int NWG = 256, NTHR = 512;
constexpr int QN = NH * DH;

constexpr int WS_EPOCH = 0;
constexpr int WS_S1 = 4096;
constexpr int WS_ML = WS_S1 + 240 * 64 * 8;
constexpr int WS_OP = WS_ML + 256 * 128;
constexpr int FIN_STRIDE = 72 * 128;
constexpr int WS_FIN = WS_OP + 256 * 192 * 8;
constexpr int WS_END = WS_FIN + 40 * FIN_STRIDE;
#define STAMP(i) do { } while (0)
#define STAMP0() do { } while (0)
#define STAMP_DECL do { } while (0)
#define SETX(i, v) do { } while (0)
#define STAMP_FLUSH() do { } while (0)

constexpr int AUX_SC1 = 16;
#define DEPTH 3
#define WO_LAST
#define SLEEP1 "1"
#define STAG2 "5"
#define TSLEEP 1
#define TAIL_SLEEP() do { if (TSLEEP) __builtin_amdgcn_s_sleep(TSLEEP); } while (0)
constexpr unsigned SPIN_CAP = 1u << 18;

#define SB() __builtin_amdgcn_sched_barrier(0)
#define CFENCE() asm volatile("" ::: "memory")
#define WG_BARRIER() do { asm volatile("s_waitcnt lgkmcnt(0)" ::: "memory"); __builtin_amdgcn_s_barrier(); asm volatile("" ::: "memory"); } while (0)

#define AUX_STREAM 2
typedef __amdgpu_buffer_rsrc_t rsrc_t;
__device__ __forceinline__ rsrc_t mkrs(const void* p, unsigned bytes) { return __builtin_amdgcn_make_buffer_rsrc((void*)p, 0, bytes, 0x00020000); }
__device__ __forceinline__ v4u ldst(rsrc_t rs, unsigned voff, unsigned soff) { return __builtin_amdgcn_raw_buffer_load_b128(rs, voff, soff, AUX_STREAM); }
#define POLL_LD(dst, voff, base, imm) asm volatile("global_load_dwordx4 %0, %1, %2 offset:" #imm " sc1" : "=v"(dst) : "v"(voff), "s"(base) : "memory")
__device__ __forceinline__ float dot2(unsigned k, unsigned q, float acc) {
  return __builtin_amdgcn_fdot2_f32_bf16(__builtin_bit_cast(bf16x2, k), __builtin_bit_cast(bf16x2, q), acc, false);
}
__device__ __forceinline__ unsigned cvtpk(float lo, float hi) {
  bf16x2 v = {(__bf16)lo, (__bf16)hi};
  return __builtin_bit_cast(unsigned, v);
}
__device__ __forceinline__ float asf(unsigned u) { return __builtin_bit_cast(float, u); }
__device__ __forceinline__ unsigned asu(float f) { return __builtin_bit_cast(unsigned, f); }
__device__ __forceinline__ float bfr(float f) { return asf(cvtpk(f, f) << 16); }
__device__ __forceinline__ float bflo(unsigned u) { return asf(u << 16); }
__device__ __forceinline__ float bfhi(unsigned u) { return asf(u & 0xffff0000u); }
template <int CTRL> __device__ __forceinline__ float dpp(float x) {
  return __builtin_bit_cast(float, __builtin_amdgcn_mov_dpp(__builtin_bit_cast(int, x), CTRL, 0xf, 0xf, true));
}
constexpr int XOR1 = 0xB1, XOR2 = 0x4E, HMIR = 0x141, ROR8 = 0x128, ROR4 = 0x124;
__device__ __forceinline__ float sum8(float x) { x += dpp<XOR1>(x); x += dpp<XOR2>(x); x += dpp<HMIR>(x); return x; }
__device__ __forceinline__ float max8(float x) { x = fmaxf(x, dpp<XOR1>(x)); x = fmaxf(x, dpp<XOR2>(x)); x = fmaxf(x, dpp<HMIR>(x)); return x; }
__device__ __forceinline__ float sum_hi3(float x) {
  x += dpp<ROR8>(x);
  auto s = __builtin_amdgcn_permlane16_swap(asu(x), asu(x), false, false);
  x = asf(s[0]) + asf(s[1]);
  auto t = __builtin_amdgcn_permlane32_swap(asu(x), asu(x), false, false);
  return asf(t[0]) + asf(t[1]);
}
__device__ __forceinline__ float max_hi3(float x) {
  x = fmaxf(x, dpp<ROR8>(x));
  auto s = __builtin_amdgcn_permlane16_swap(asu(x), asu(x), false, false);
  x = fmaxf(asf(s[0]), asf(s[1]));
  auto t = __builtin_amdgcn_permlane32_swap(asu(x), asu(x), false, false);
  return fmaxf(asf(t[0]), asf(t[1]));
}
__device__ __forceinline__ f32x4 mfma(v4u a, v4u b, f32x4 c) {
  return __builtin_amdgcn_mfma_f32_16x16x32_bf16(__builtin_bit_cast(bf16x8, a), __builtin_bit_cast(bf16x8, b), c, 0, 0, 0);
}
__device__ __forceinline__ v4u sel(unsigned pp, const v4u& m) { return v4u{pp & m[0], pp & m[1], pp & m[2], pp & m[3]}; }
template <class T> __device__ __forceinline__ unsigned ldsaddr(T* p) { return (unsigned)(size_t)(LAS T*)p; }
template <int ROW> __device__ __forceinline__ f32x2 xw_sum(unsigned p) {
  const f32x2 s = (*LP(const f32x2, p) + *LP(const f32x2, p + ROW * 4)) + (*LP(const f32x2, p + ROW * 8) + *LP(const f32x2, p + ROW * 12));
  auto a = __builtin_amdgcn_permlane16_swap(asu(s[0]), asu(s[0]), false, false);
  auto c = __builtin_amdgcn_permlane16_swap(asu(s[1]), asu(s[1]), false, false);
  const float u = asf(a[0]) + asf(a[1]), v = asf(c[0]) + asf(c[1]);
  auto d = __builtin_amdgcn_permlane32_swap(asu(u), asu(u), false, false);
  auto e = __builtin_amdgcn_permlane32_swap(asu(v), asu(v), false, false);
  return f32x2{asf(d[0]) + asf(d[1]), asf(e[0]) + asf(e[1])};
}
__device__ __forceinline__ v4u gran2(float v0, float v1, unsigned tagA, unsigned tagB) {
  return v4u{(asu(v0) & 0xffffff00u) | tagB, tagA, (asu(v1) & 0xffffff00u) | tagB, tagA};
}
__device__ __forceinline__ unsigned gbad(const v4u& g, unsigned tagA, unsigned tagB) {
  return (g[1] ^ tagA) | (g[3] ^ tagA) | (((g[0] ^ tagB) | (g[2] ^ tagB)) & 0xffu);
}
__device__ __forceinline__ bool any_bad(unsigned bad) { return __builtin_amdgcn_ballot_w64(bad != 0u) != 0ull; }
__device__ __forceinline__ float gval(unsigned u) { return asf(u & 0xffffff00u); }

constexpr int I_W2 = 5, I_K = 6, I_V = 30, I_L = 54, I_END = 58;
#define D_INIT DEPTH
#define D_BAR DEPTH
#define D_END 4
constexpr int depth_at(int i) { return i < 0 ? D_INIT : (i == 4 || i == 17 || i == I_V) ? D_BAR : i >= I_END - 5 - 1 ? D_END : DEPTH; }
constexpr int upto(int i) { int u = -1; for (int k = -1; k <= i; ++k) { const int t = k + depth_at(k) < I_END - 1 ? k + depth_at(k) : I_END - 1; u = t > u ? t : u; } return u; }
constexpr int RA = 200, RB = 136, RS = 72;
struct alignas(16) Lds {
  float redA[16 * RA];
  float redB[16 * RB];
  float red1[16 * RS];
  float red2[16 * RS];
  unsigned qh[128];
  float wm[8];
  float wl[8];
  u16 xs[640];
};

__global__ void __launch_bounds__(NTHR, 2)
fused_decode_kernel(const u16*, const u16*, const u16*, const u16*, const u16*, const u16*, const u16*, const u16*, const u16*, u16*, unsigned char*) {
  typedef unsigned v16u __attribute__((ext_vector_type(16)));
  v16u ka; v4u kb; u64 kw;
  int b = blockIdx.x;
  asm volatile("s_load_dwordx16 %0, %4, 0x0\n\ts_load_dwordx4 %1, %4, 0x40\n\ts_load_dwordx2 %2, %4, 0x50"
               : "=&s"(ka), "=&s"(kb), "=&s"(kw), "+s"(b) : "s"(__builtin_amdgcn_kernarg_segment_ptr()));
  SB();
  __shared__ Lds L;
  const u64 tpad0 = __builtin_amdgcn_s_memrealtime();
  STAMP_DECL;
  STAMP0();
  asm volatile("" ::: "v187");
  const int tid = threadIdx.x, lane = tid & 63, w = __builtin_amdgcn_readfirstlane(tid >> 6), r = lane >> 3, c = lane & 7;
  const int h = (b & 7) * 4 + ((b >> 3) & 3), j = b >> 5;
  const bool isq = j < 6;
  const int u1 = (j == 6) ? h : 32 + h;
  const bool has1 = isq || u1 < 48;
  const int mq = (j - 6) >> 31;
  int cg = ((3 * h + (j >> 1)) & mq) | ((96 + (u1 >> 1)) & ~mq), half = ((j & mq) | (u1 & ~mq)) & 1;
  unsigned stride = FF * 2 + ((QN * 2 - FF * 2) & mq);
  unsigned o1 = (cg - (96 & ~mq)) * 128, n1 = has1 ? DM * stride : 0u;
  unsigned off1 = (half * 320 + w * 8 + r) * stride + c * 16;
  unsigned kvrow = h * SK + (SK - WIN) + (w * 8 + j) * 8;
  constexpr int BSTR = 512 * DH * 2;
  u64 kvoff = (u64)kvrow * (DH * 2);
  unsigned loff = (unsigned)(r * (DH * 2) + c * 16);
  const int vu = b < 240 ? b : 239;
  int rg = vu / 10, cgrp = vu - rg * 10;
  unsigned o2 = (rg * 64 + w * 8) * (DM * 2) + cgrp * 128;
  const int P = h >> 1, jj = (h & 1) * 8 + j;
  int rgp = jj / 5, cg5 = jj - rgp * 5;
  const bool isred = jj == 15;
  const int rowb = 128 * rgp + 16 * w;
  unsigned ol = (P * 384 + rowb) * (DM * 2) + cg5 * 256;
  unsigned offl = (lane >> 4) * (4 * DM * 2) + (lane & 15) * 16, off2 = r * (DM * 2) + c * 16, offx = lane * 16, offx2 = (lane & 15) * 16;
  asm volatile("" : "+s"(cg), "+s"(half), "+s"(stride), "+s"(o1), "+s"(n1), "+s"(kvoff), "+s"(rg), "+s"(cgrp), "+s"(o2), "+s"(rgp), "+s"(cg5), "+s"(ol));
  asm volatile("" : "+v"(off1), "+v"(loff), "+v"(offl), "+v"(off2), "+v"(offx), "+v"(offx2));
  SB();
  asm volatile("s_waitcnt lgkmcnt(0)" : "+s"(ka), "+s"(kb), "+s"(kw));
#define KARG(v, i) ((const unsigned char*)(((u64)(v)[2 * (i) + 1] << 32) | (v)[2 * (i)]))
  const unsigned char *x_ = KARG(ka, 0), *kc_ = KARG(ka, 1), *vc_ = KARG(ka, 2), *wq_ = KARG(ka, 3), *wo_ = KARG(ka, 4), *w1_ = KARG(ka, 5), *b1_ = KARG(ka, 6),
                      *w2_ = KARG(ka, 7), *b2_ = KARG(kb, 0), *out_ = KARG(kb, 1), *ws_ = (const unsigned char*)kw;
  GAS unsigned char* ws = (GAS unsigned char*)ws_;
  const GAS unsigned char* x = (const GAS unsigned char*)x_;
  const rsrc_t rw1 = mkrs((const unsigned char*)(isq ? wq_ : w1_) + o1, n1);
  rsrc_t RW2, RWO, RK, RV;
  v4u R[I_END];
#define ISSUE(i) do { if constexpr ((i) < I_W2) R[i] = ldst(rw1, off1, (i) * 64 * stride); \
    else if constexpr ((i) == I_W2) R[i] = ldst(RW2, off2, 0); \
    else if constexpr ((i) >= I_K && (i) < I_K + 24) R[i] = ldst(RK, loff + (((i) - I_K) % 3) * 128, (((i) - I_K) / 3) * BSTR); \
    else if constexpr ((i) >= I_V && (i) < I_V + 24) R[i] = ldst(RV, loff + (((i) - I_V) % 3) * 128, (((i) - I_V) / 3) * BSTR); \
    else if constexpr ((i) >= I_L && (i) < I_L + 4) R[i] = ldst(RWO, offl + ((i) - I_L) * (DM * 2), 0); \
    SB(); } while (0)
#define TOUCH(i) do { asm volatile("" : "+v"(R[i])); SB(); } while (0)
#define STEP(i) do { constexpr int u0_ = upto((i) - 1), u1_ = upto(i); TOUCH(i); if constexpr (u0_ < u1_) ISSUE(u0_ + 1); if constexpr (u0_ + 1 < u1_) ISSUE(u0_ + 2); \
    if constexpr (u0_ + 2 < u1_) ISSUE(u0_ + 3); } while (0)
#define STEP3(i) do { STEP(i); STEP((i) + 1); STEP((i) + 2); } while (0)

  const v4u xa = *(const GAS v4u*)(x + offx);
  const v4u xb = *(const GAS v4u*)(x + 1024 + offx2);
  SB();
  ISSUE(0); ISSUE(1);
  if constexpr (upto(-1) >= 2) ISSUE(2);
  if constexpr (upto(-1) >= 3) ISSUE(3);
  static_assert(upto(-1) <= 3 && upto(I_V + 23) == I_END - 1, "issue schedule");
  u64 ep;
  asm volatile("s_load_dwordx2 %0, %1, 0x0" : "=s"(ep) : "s"(ws_) : "memory");
  SB();
  STAMP(1);
  SETX(15, (unsigned)__builtin_amdgcn_s_getreg(20 | (3 << 11)) & 0xfu);
  const rsrc_t rs = mkrs(ws_, WS_END);
  RW2 = mkrs((const unsigned char*)w2_ + o2, b < 240 ? 8 * DM * 2 : 0);
  RWO = mkrs((const unsigned char*)wo_ + ol, isred ? 0 : 16 * DM * 2);
  RK = mkrs((const unsigned char*)kc_ + kvoff, 7 * BSTR + 8 * DH * 2);
  RV = mkrs((const unsigned char*)vc_ + kvoff, 7 * BSTR + 8 * DH * 2);

  v4u msk;
#pragma unroll
  for (int d = 0; d < 4; ++d) msk[d] = ((c >> 1) == d) ? ((c & 1) ? 0xffff0000u : 0x0000ffffu) : 0u;

  STEP(0); STEP(1);
  if constexpr (D_INIT >= 3) STEP(2);
  if constexpr (D_INIT >= 4) STEP(3);
  float mean_b, rden;
  unsigned xr[5];
  {
    const bool hb = lane < 16;
    *(v4u*)&L.xs[lane * 8] = xa;
    if (hb) *(v4u*)&L.xs[512 + lane * 8] = xb;
#pragma unroll
    for (int p = 0; p < 5; ++p) xr[p] = L.xs[half * 320 + p * 64 + w * 8 + r];
    float e[16];
#pragma unroll
    for (int k = 0; k < 4; ++k) { e[2 * k] = bflo(xa[k]); e[2 * k + 1] = bfhi(xa[k]); e[8 + 2 * k] = bflo(xb[k]); e[9 + 2 * k] = bfhi(xb[k]); }
    float s = 0.f, s2 = 0.f;
#pragma unroll
    for (int k = 0; k < 8; ++k) { s += e[k]; s2 += e[8 + k]; }
    s += hb ? s2 : 0.f;
    s = sum_hi3(sum8(s));
    const float mean = s * (1.f / DM);
    float v = 0.f, v2 = 0.f;
#pragma unroll
    for (int k = 0; k < 8; ++k) { const float d0 = e[k] - mean, d1 = e[8 + k] - mean; v = fmaf(d0, d0, v); v2 = fmaf(d1, d1, v2); }
    v += hb ? v2 : 0.f;
    v = sum_hi3(sum8(v));
    mean_b = bfr(mean);
    rden = __builtin_amdgcn_rcpf(bfr(__builtin_amdgcn_sqrtf(bfr(bfr(v * (1.f / DM)) + 1e-5f))));
  }
  SB();
#define PRE_STEPS() do { } while (0)
#define MID_STEPS() do { if constexpr (D_INIT < 3) STEP(2); if constexpr (D_INIT < 4) STEP(3); } while (0)
  const bool dvalid = ((lane >> 3) & 1) == (lane >> 5);
  const int drow = w * 2 + (lane >> 5), dpos = c * 8 + ((lane >> 4) & 1) * 4;
  const int l16 = lane & 15;
  {
    v4u a1[5];
#pragma unroll
    for (int p = 0; p < 5; ++p) { const float xn = bfr(bflo(xr[p]) - mean_b) * rden; a1[p] = sel(cvtpk(xn, xn), msk); }
    SB();
    MID_STEPS(); STEP(4);
    f32x4 acc = {0.f, 0.f, 0.f, 0.f};
#pragma unroll
    for (int p = 0; p < 5; ++p) acc = mfma(a1[p], R[p], acc);
    if (dvalid) *(f32x4*)&L.red1[drow * RS + dpos] = acc;
  }
  WG_BARRIER();
  asm volatile("" : "+s"(ep));
  const u64 tg = ep + 1;
  const unsigned tagA = (unsigned)tg, tagH = (unsigned)(tg >> 32), tagB = tagH & 0xffu;
  if (w >= 6 && has1) {
    const int pr = (w - 6) * 16 + l16;
    const f32x2 s = xw_sum<RS>(ldsaddr(L.red1) + ((lane >> 4) * (4 * RS) + 2 * pr) * 4);
    if (lane < 16) __builtin_amdgcn_raw_buffer_store_b128(gran2(s[0], s[1], tagA, tagB), rs, WS_S1 + (cg * 2 + half) * 512 + pr * 16, 0, AUX_SC1);
  }
  SB();
  STAMP(2);
  STEP(5); STEP3(6); STEP3(9); STEP3(12);
  v4u g0, g1; unsigned b1p;
  const unsigned goff = WS_S1 + (tid < 96 ? (3 * h + (tid >> 5)) * 1024 + (tid & 31) * 16 : (96 + rg) * 1024 + (tid & 31) * 16);
  if (w < 2) {
    POLL_LD(g0, goff, ws, 0); POLL_LD(g1, goff, ws, 512);
    asm volatile("global_load_dword %0, %1, %2" : "=v"(b1p) : "v"(rg * 128 + (lane & 31) * 4), "s"(b1_) : "memory");
  }
  SB();
  STEP3(15);
  if (w < 2) {
    asm volatile("s_waitcnt vmcnt(%3)" : "+v"(g0), "+v"(g1), "+v"(b1p) : "n"(upto(17) - upto(14)) : "memory");
    unsigned spins = 0;
    while (any_bad(gbad(g0, tagA, tagB) | gbad(g1, tagA, tagB))) {
      if (++spins > SPIN_CAP) break;
      __builtin_amdgcn_s_sleep(1);
      asm volatile("global_load_dwordx4 %0, %2, %3 sc1\n\tglobal_load_dwordx4 %1, %2, %3 offset:512 sc1\n\ts_waitcnt vmcnt(0)"
                   : "=&v"(g0), "=&v"(g1) : "v"(goff), "s"(ws) : "memory");
    }
    SETX(12, spins);
    float a0 = gval(g0[0]) + gval(g1[0]), a1 = gval(g0[2]) + gval(g1[2]);
    if (tid >= 96) {
      a0 = fmaxf(bfr(bfr(a0) + bflo(b1p)), 0.f); a1 = fmaxf(bfr(bfr(a1) + bfhi(b1p)), 0.f);
      a0 *= a0; a1 *= a1;
    }
    L.qh[tid] = cvtpk(a0, a1);
  }
  WG_BARRIER();
  STAMP(5);
  float sc[8];
  {
    v4u q[3];
#pragma unroll
    for (int i = 0; i < 3; ++i) q[i] = *(const v4u*)&L.qh[i * 32 + c * 4];
    const unsigned hv = ((const u16*)&L.qh[96])[w * 8 + r];
    constexpr float C2 = 0.07216878364870322f * 1.4426950408889634f;
#define QK(bk) do { float d_ = 0.f; _Pragma("unroll") for (int i = 0; i < 3; ++i) _Pragma("unroll") for (int k = 0; k < 4; ++k) d_ = dot2(R[I_K + 3 * (bk) + i][k], q[i][k], d_); \
    sc[bk] = sum8(d_) * C2; SB(); } while (0)
#define W2MM() do { const f32x4 acc_ = mfma(sel(hv | (hv << 16), msk), R[I_W2], f32x4{0.f, 0.f, 0.f, 0.f}); if (dvalid) *(f32x4*)&L.red2[drow * RS + dpos] = acc_; SB(); } while (0)
    STEP(18); QK(0); STEP(19); QK(1); STEP(20); QK(2); STEP(21); QK(3); STEP(22); QK(4); STEP(23); QK(5);
    STEP(24); W2MM(); STEP(25); STEP(26); QK(6); STEP3(27); QK(7);
  }
  {
    float m = sc[0];
#pragma unroll
    for (int bk = 1; bk < 8; ++bk) m = fmaxf(m, sc[bk]);
    m = max_hi3(m);
    float l = 0.f;
#pragma unroll
    for (int bk = 0; bk < 8; ++bk) l += __builtin_amdgcn_exp2f(sc[bk] - m);
    l = sum_hi3(l);
    if (lane == 0) { L.wm[w] = m; L.wl[w] = l; }
  }
  SB();
  STEP(I_V);
  WG_BARRIER();
  const float mw = L.wm[c], M = max8(mw);
  if (w == 5) {
    const float l = sum8(__builtin_amdgcn_exp2f(mw - M) * L.wl[c]);
    if (lane < 8) __builtin_amdgcn_raw_buffer_store_b128(v4u{asu(M), tagA, (asu(l) & 0xffffff00u) | tagB, tagA}, rs, WS_ML + (h * 8 + j) * 128 + lane * 16, 0, AUX_SC1);
  }
  SB();
  v4u A[8];
#pragma unroll
  for (int bk = 0; bk < 8; ++bk) { const float p = __builtin_amdgcn_exp2f(sc[bk] - M); A[bk] = sel(cvtpk(p, p), msk); }
  SB();
  STEP(I_V + 1);
  if (w >= 6 && b < 240) {
    const int pr = (w - 6) * 16 + l16;
    const f32x2 s = xw_sum<RS>(ldsaddr(L.red2) + ((lane >> 4) * (4 * RS) + 2 * pr) * 4);
    if (lane < 16) __builtin_amdgcn_raw_buffer_store_b128(gran2(s[0], s[1], tagA, tagB), rs, WS_FIN + (cgrp * 4 + (pr >> 3)) * FIN_STRIDE + (48 + rg) * 128 + (pr & 7) * 16, 0, AUX_SC1);
  }
  SB();
  STAMP(6);
  float F;
  const int prw = w * 16 + l16;
  unsigned aA = ldsaddr(L.redA) + ((lane >> 4) * (4 * RA) + 2 * prw) * 4, aAw = ldsaddr(L.redA) + (drow * RA + dpos) * 4;
  unsigned aB = ldsaddr(L.redB) + ((lane >> 4) * (4 * RB) + 2 * prw) * 4, aBw = ldsaddr(L.redB) + (drow * RB + l16 * 8 + ((lane >> 4) & 1) * 4) * 4;
  unsigned sA = WS_OP + (h * 8 + j) * 1536 + prw * 16;
  unsigned sB = WS_FIN + (cg5 * 8 + (prw >> 3)) * FIN_STRIDE + (P * 3 + rgp) * 128 + (prw & 7) * 16;
  unsigned ooff = WS_OP + ((2 * P + (rowb >= DH ? 1 : 0)) * 8 + c) * 1536 + (rowb >= DH ? rowb - DH : rowb) * 8 + r * 16;
  unsigned vff = 0xffu;
  const v4u z4 = {0u, 0u, 0u, 0u};
  v4u mk0 = ((lane >> 3) & 1) ? z4 : msk, mk1 = ((lane >> 3) & 1) ? msk : z4;
  {
    v4u gm;
    const unsigned moff = WS_ML + (h * 8 + c) * 128 + r * 16;
    constexpr int NAFTER = upto(I_V + 20) - upto(I_V + 17);
    f32x4 acc[3] = {{0.f, 0.f, 0.f, 0.f}, {0.f, 0.f, 0.f, 0.f}, {0.f, 0.f, 0.f, 0.f}};
#define PV(i) do { acc[(i) % 3] = mfma(A[(i) / 3], R[I_V + (i)], acc[(i) % 3]); SB(); } while (0)
#define SPV(i) do { STEP(I_V + (i)); PV(i); } while (0)
#define SPV3(i) do { SPV(i); SPV((i) + 1); SPV((i) + 2); } while (0)
    PV(0); PV(1); SPV(2); SPV3(3); SPV3(6); SPV3(9); SPV3(12); SPV3(15);
    POLL_LD(gm, moff, ws, 0);
    SB();
    SPV3(18);
    asm volatile("s_waitcnt vmcnt(%1)" : "+v"(gm) : "n"(NAFTER) : "memory");
    {
      unsigned spins = 0;
      while (any_bad((gm[1] ^ tagA) | (gm[3] ^ tagA) | ((gm[2] ^ tagB) & 0xffu))) {
        if (++spins > SPIN_CAP) break;
        __builtin_amdgcn_s_sleep(2);
        asm volatile("global_load_dwordx4 %0, %1, %2 sc1\n\ts_waitcnt vmcnt(0)" : "=&v"(gm) : "v"(moff), "s"(ws) : "memory");
      }
      const float mc = asf(gm[0]), Mh = max8(mc);
      F = __builtin_amdgcn_exp2f(M - Mh) * __builtin_amdgcn_rcpf(sum8(__builtin_amdgcn_exp2f(mc - Mh) * gval(gm[2])));
    }
    asm volatile("" : "+v"(F), "+v"(aA), "+v"(aAw), "+v"(sA), "+v"(ooff), "+v"(aB), "+v"(aBw), "+v"(sB), "+v"(mk0), "+v"(mk1), "+v"(vff));
    SB();
    SPV3(21);
    if (dvalid) {
#pragma unroll
      for (int i = 0; i < 3; ++i) *LP(f32x4, aAw + i * 256) = acc[i];
    }
  }
  STAMP(7);
  SETX(14, __builtin_amdgcn_s_memtime() - cyc0);
  WG_BARRIER();
  STAMP(3);
  if (w < 6) {
    const f32x2 s = xw_sum<RA>(aA);
    if (lane < 16) __builtin_amdgcn_raw_buffer_store_b128(gran2(s[0] * F, s[1] * F, tagA, tagB), rs, sA, 0, AUX_SC1);
  }
  SB();
  STAMP(4);
  if (!isred) {
    float o0, o1;
    unsigned spins = SPIN_CAP;
    u64 sv;
    asm volatile(
      "s_mov_b64 %[sv], exec\n\t"
      "1:\n\t"
      "global_load_dwordx4 v[128:131], %[off], %[base] sc1\n\t"
      "s_waitcnt vmcnt(0)\n\t"
      "v_bitop3_b32 v184, v129, v131, %[ta] bitop3:0x7e\n\tv_bitop3_b32 v185, v128, v130, %[tb] bitop3:0x7e\n\t"
      "v_and_or_b32 v184, v185, %[ff], v184\n\tv_cmp_ne_u32 vcc, 0, v184\n\t"
      "s_and_b64 exec, exec, vcc\n\t"
      "s_cbranch_scc0 2f\n\t"
      "s_sleep " SLEEP1 "\n\t"
      "s_sub_u32 %[n], %[n], 1\n\ts_cbranch_scc0 1b\n\t"
      "2:\n\t"
      "s_mov_b64 exec, %[sv]\n\t"
      "v_and_b32 %[o0], %[mk], v128\n\tv_and_b32 %[o1], %[mk], v130\n\ts_nop 1"
      : [o0] "=&v"(o0), [o1] "=&v"(o1), [n] "+s"(spins), [sv] "=&s"(sv)
      : [off] "v"(ooff), [base] "s"(ws), [ta] "s"(tagA), [tb] "s"(tagB), [ff] "v"(vff), [mk] "s"(0xffffff00u)
      : "memory", "vcc", "scc", "v128", "v129", "v130", "v131", "v184", "v185");
    STAMP(8);
    SETX(13, SPIN_CAP - spins);
    o0 = sum8(o0); o1 = sum8(o1);
    const unsigned p0 = cvtpk(o0, o0), p1 = cvtpk(o1, o1);
    f32x4 acc = mfma(sel(p0, mk0), R[I_L], f32x4{0.f, 0.f, 0.f, 0.f});
    acc = mfma(sel(p1, mk0), R[I_L + 1], acc);
    acc = mfma(sel(p0, mk1), R[I_L + 2], acc);
    acc = mfma(sel(p1, mk1), R[I_L + 3], acc);
    *LP(f32x4, aBw) = acc;
    WG_BARRIER();
    if (w < 4) {
      const f32x2 s = xw_sum<RB>(aB);
      if (lane < 16) __builtin_amdgcn_raw_buffer_store_b128(gran2(s[0], s[1], tagA, tagB), rs, sB, 0, AUX_SC1);
    }
    STAMP(9);
  } else if (P + 16 * w < 40) {
    const int fl = P + 16 * w;
    const unsigned xp = *(const GAS unsigned*)(x + fl * 32 + c * 4), b2p = *(const GAS unsigned*)((const GAS unsigned char*)b2_ + fl * 32 + c * 4);
    const unsigned foff = WS_FIN + fl * FIN_STRIDE + 4096 + lane * 16;
    v4u a[6];
    unsigned spins = 0;
    for (;;) {
      asm volatile("global_load_dwordx4 %0, %3, %4 offset:2048 sc1\n\tglobal_load_dwordx4 %1, %3, %4 offset:3072 sc1\n\tglobal_load_dwordx4 %2, %5, %4 sc1\n\ts_waitcnt vmcnt(0)"
                   : "=&v"(a[0]), "=&v"(a[1]), "=&v"(a[2]) : "v"(foff), "s"(ws), "v"(foff + 4096) : "memory");
      const unsigned bad = gbad(a[0], tagA, tagB) | gbad(a[1], tagA, tagB) | gbad(a[2], tagA, tagB);
      if (!any_bad(bad) || ++spins > SPIN_CAP) break;
      __builtin_amdgcn_s_sleep(1);
    }
    const float O0 = sum_hi3((gval(a[0][0]) + gval(a[1][0])) + gval(a[2][0])), O1 = sum_hi3((gval(a[0][2]) + gval(a[1][2])) + gval(a[2][2]));
    const float t0 = bfr(bfr(O0) + bflo(b2p)), t1 = bfr(bfr(O1) + bfhi(b2p));
    constexpr float L2E = 1.4426950408889634f;
    float ga = bfr(t0 * __builtin_amdgcn_rcpf(1.f + __builtin_amdgcn_exp2f(-L2E * t0)));
    float gb = bfr(t1 * __builtin_amdgcn_rcpf(1.f + __builtin_amdgcn_exp2f(-L2E * t1)));
    GAS unsigned* op = (GAS unsigned*)((GAS unsigned char*)out_ + fl * 32 + c * 4);
    float x0 = bflo(xp), x1 = bfhi(xp);
    asm volatile("" : "+v"(ga), "+v"(gb), "+v"(op), "+v"(x0), "+v"(x1));
    STAMP(9);
    float sa[6], sb[6];
    spins = SPIN_CAP;
    asm volatile(
      "global_load_dwordx4 v[128:131], %[off], %[base] offset:-4096 sc1\n\t"
      "global_load_dwordx4 v[132:135], %[off], %[base] offset:-3072 sc1\n\t"
      "global_load_dwordx4 v[136:139], %[off], %[base] offset:-2048 sc1\n\t"
      "global_load_dwordx4 v[140:143], %[off], %[base] offset:-1024 sc1\n\t"
      "global_load_dwordx4 v[144:147], %[off], %[base] offset:0 sc1\n\t"
      "global_load_dwordx4 v[148:151], %[off], %[base] offset:1024 sc1\n\t"
      "s_sleep " STAG2 "\n\t"
      "1:\n\t"
      "global_load_dwordx4 v[152:155], %[off], %[base] offset:-4096 sc1\n\t"
      "global_load_dwordx4 v[156:159], %[off], %[base] offset:-3072 sc1\n\t"
      "global_load_dwordx4 v[160:163], %[off], %[base] offset:-2048 sc1\n\t"
      "global_load_dwordx4 v[164:167], %[off], %[base] offset:-1024 sc1\n\t"
      "global_load_dwordx4 v[168:171], %[off], %[base] offset:0 sc1\n\t"
      "global_load_dwordx4 v[172:175], %[off], %[base] offset:1024 sc1\n\t"
      "s_waitcnt vmcnt(6)\n\t"
      "v_bitop3_b32 v176, v129, v131, %[ta] bitop3:0x7e\n\tv_bitop3_b32 v182, v128, v130, %[tb] bitop3:0x7e\n\t"
      "v_bitop3_b32 v177, v133, v135, %[ta] bitop3:0x7e\n\tv_bitop3_b32 v183, v132, v134, %[tb] bitop3:0x7e\n\t"
      "v_bitop3_b32 v178, v137, v139, %[ta] bitop3:0x7e\n\tv_bitop3_b32 v184, v136, v138, %[tb] bitop3:0x7e\n\t"
      "v_bitop3_b32 v179, v141, v143, %[ta] bitop3:0x7e\n\tv_bitop3_b32 v185, v140, v142, %[tb] bitop3:0x7e\n\t"
      "v_bitop3_b32 v180, v145, v147, %[ta] bitop3:0x7e\n\tv_bitop3_b32 v186, v144, v146, %[tb] bitop3:0x7e\n\t"
      "v_bitop3_b32 v181, v149, v151, %[ta] bitop3:0x7e\n\tv_bitop3_b32 v187, v148, v150, %[tb] bitop3:0x7e\n\t"
      "v_or3_b32 v176, v176, v177, v178\n\tv_or3_b32 v182, v182, v183, v184\n\tv_or3_b32 v179, v179, v180, v181\n\tv_or3_b32 v182, v182, v185, v186\n\t"
      "v_or_b32 v182, v182, v187\n\tv_or_b32 v176, v176, v179\n\tv_and_or_b32 v176, v182, %[ff], v176\n\tv_cmp_ne_u32 vcc, 0, v176\n\t"
      "s_cbranch_vccz 2f\n\t"
      "global_load_dwordx4 v[128:131], %[off], %[base] offset:-4096 sc1\n\t"
      "global_load_dwordx4 v[132:135], %[off], %[base] offset:-3072 sc1\n\t"
      "global_load_dwordx4 v[136:139], %[off], %[base] offset:-2048 sc1\n\t"
      "global_load_dwordx4 v[140:143], %[off], %[base] offset:-1024 sc1\n\t"
      "global_load_dwordx4 v[144:147], %[off], %[base] offset:0 sc1\n\t"
      "global_load_dwordx4 v[148:151], %[off], %[base] offset:1024 sc1\n\t"
      "s_waitcnt vmcnt(6)\n\t"
      "v_bitop3_b32 v176, v153, v155, %[ta] bitop3:0x7e\n\tv_bitop3_b32 v182, v152, v154, %[tb] bitop3:0x7e\n\t"
      "v_bitop3_b32 v177, v157, v159, %[ta] bitop3:0x7e\n\tv_bitop3_b32 v183, v156, v158, %[tb] bitop3:0x7e\n\t"
      "v_bitop3_b32 v178, v161, v163, %[ta] bitop3:0x7e\n\tv_bitop3_b32 v184, v160, v162, %[tb] bitop3:0x7e\n\t"
      "v_bitop3_b32 v179, v165, v167, %[ta] bitop3:0x7e\n\tv_bitop3_b32 v185, v164, v166, %[tb] bitop3:0x7e\n\t"
      "v_bitop3_b32 v180, v169, v171, %[ta] bitop3:0x7e\n\tv_bitop3_b32 v186, v168, v170, %[tb] bitop3:0x7e\n\t"
      "v_bitop3_b32 v181, v173, v175, %[ta] bitop3:0x7e\n\tv_bitop3_b32 v187, v172, v174, %[tb] bitop3:0x7e\n\t"
      "v_or3_b32 v176, v176, v177, v178\n\tv_or3_b32 v182, v182, v183, v184\n\tv_or3_b32 v179, v179, v180, v181\n\tv_or3_b32 v182, v182, v185, v186\n\t"
      "v_or_b32 v182, v182, v187\n\tv_or_b32 v176, v176, v179\n\tv_and_or_b32 v176, v182, %[ff], v176\n\tv_cmp_ne_u32 vcc, 0, v176\n\t"
      "s_cbranch_vccz 3f\n\t"
      "s_sub_u32 %[n], %[n], 1\n\ts_cbranch_scc0 1b\n\t"
      "3:\n\t"
      "v_and_b32 %[a0], %[mk], v152\n\tv_and_b32 %[b0], %[mk], v154\n\t"
      "v_and_b32 %[a1], %[mk], v156\n\tv_and_b32 %[b1], %[mk], v158\n\t"
      "v_and_b32 %[a2], %[mk], v160\n\tv_and_b32 %[b2], %[mk], v162\n\t"
      "v_and_b32 %[a3], %[mk], v164\n\tv_and_b32 %[b3], %[mk], v166\n\t"
      "v_and_b32 %[a4], %[mk], v168\n\tv_and_b32 %[b4], %[mk], v170\n\t"
      "v_and_b32 %[a5], %[mk], v172\n\tv_and_b32 %[b5], %[mk], v174\n\t"
      "s_branch 5f\n\t"
      "2:\n\t"
      "v_and_b32 %[a0], %[mk], v128\n\tv_and_b32 %[b0], %[mk], v130\n\t"
      "v_and_b32 %[a1], %[mk], v132\n\tv_and_b32 %[b1], %[mk], v134\n\t"
      "v_and_b32 %[a2], %[mk], v136\n\tv_and_b32 %[b2], %[mk], v138\n\t"
      "v_and_b32 %[a3], %[mk], v140\n\tv_and_b32 %[b3], %[mk], v142\n\t"
      "v_and_b32 %[a4], %[mk], v144\n\tv_and_b32 %[b4], %[mk], v146\n\t"
      "v_and_b32 %[a5], %[mk], v148\n\tv_and_b32 %[b5], %[mk], v150\n\t"
      "5:\n\ts_nop 1"
      : [a0] "=&v"(sa[0]), [b0] "=&v"(sb[0]), [a1] "=&v"(sa[1]), [b1] "=&v"(sb[1]), [a2] "=&v"(sa[2]), [b2] "=&v"(sb[2]), [a3] "=&v"(sa[3]), [b3] "=&v"(sb[3]), [a4] "=&v"(sa[4]), [b4] "=&v"(sb[4]), [a5] "=&v"(sa[5]), [b5] "=&v"(sb[5]), [n] "+s"(spins)
      : [off] "v"(foff), [base] "s"(ws), [ta] "s"(tagA), [tb] "s"(tagB), [ff] "v"(vff), [mk] "s"(0xffffff00u)
      : "memory", "vcc", "scc", "v128", "v129", "v130", "v131", "v132", "v133", "v134", "v135", "v136", "v137", "v138", "v139", "v140", "v141", "v142", "v143", "v144", "v145", "v146", "v147", "v148", "v149", "v150", "v151", "v152", "v153", "v154", "v155", "v156", "v157", "v158", "v159", "v160", "v161", "v162", "v163", "v164", "v165", "v166", "v167", "v168", "v169", "v170", "v171", "v172", "v173", "v174", "v175", "v176", "v177", "v178", "v179", "v180", "v181", "v182", "v183", "v184", "v185", "v186", "v187");
    STAMP(10);
    SETX(14, SPIN_CAP - spins);
    const float S0 = sum_hi3(((sa[0] + sa[1]) + (sa[2] + sa[3])) + (sa[4] + sa[5])), S1 = sum_hi3(((sb[0] + sb[1]) + (sb[2] + sb[3])) + (sb[4] + sb[5]));
    const float y0 = x0 + bfr(bfr(S0) * ga), y1 = x1 + bfr(bfr(S1) * gb);
    while (__builtin_amdgcn_s_memrealtime() - tpad0 < PAD_TO) __builtin_amdgcn_s_sleep(1);
    if (lane < 8) *op = cvtpk(y0, y1);
    if (fl == 0 && lane == 0) __hip_atomic_store((GAS u64*)(ws + WS_EPOCH), ((u64)tagH << 32) | tagA, __ATOMIC_RELAXED, __HIP_MEMORY_SCOPE_AGENT);
    STAMP(11);
  }
  STAMP_FLUSH();
}

extern "C" void kernel_launch(void* const* d_in, const int* in_sizes, int n_in, void* d_out, int out_size, void* d_ws, size_t ws_size,
                              hipStream_t stream) {
  (void)in_sizes; (void)n_in; (void)out_size; (void)ws_size;
  fused_decode_kernel<<<NWG, NTHR, 0, stream>>>((const u16*)d_in[0], (const u16*)d_in[1], (const u16*)d_in[2], (const u16*)d_in[3],
                                                (const u16*)d_in[4], (const u16*)d_in[6], (const u16*)d_in[7], (const u16*)d_in[8],
                                                (const u16*)d_in[9], (u16*)d_out, (unsigned char*)d_ws);
}
```

```cpp
#include <hip/hip_runtime.h>
#include <hip/hip_bf16.h>
#include <stdint.h>

#define GAS __attribute__((address_space(1)))
#define LAS __attribute__((address_space(3)))
#define LP(T, a) ((LAS T*)(size_t)(a))
typedef unsigned short u16;
typedef unsigned long long u64;
typedef unsigned v4u __attribute__((ext_vector_type(4)));
typedef float f32x4 __attribute__((ext_vector_type(4)));
typedef float f32x2 __attribute__((ext_vector_type(2)));
typedef short bf16x8 __attribute__((ext_vector_type(8)));
typedef __bf16 bf16x2 __attribute__((ext_vector_type(2)));

constexpr int DM = 640, NH = 32, DH = 192, SK = 32768, WIN = 4096, FF = 1536;
constexpr int NWG = 256, NTHR = 512;
constexpr int QN = NH * DH;

constexpr int WS_EPOCH = 0;
constexpr int WS_S1 = 4096;
constexpr int WS_ML = WS_S1 + 240 * 256;
constexpr int WS_OP = WS_ML + 256 * 128;
constexpr int FIN_STRIDE = 48 * 128;
constexpr int WS_FIN = WS_OP + 256 * 768;
constexpr int WS_END = WS_FIN + 20 * FIN_STRIDE;

constexpr int AUX_SC1 = 16, AUX_NT = 2;
constexpr unsigned SPIN_CAP = 1u << 18;

#define SB() __builtin_amdgcn_sched_barrier(0)
#define WG_BARRIER() do { asm volatile("s_waitcnt lgkmcnt(0)" ::: "memory"); __builtin_amdgcn_s_barrier(); asm volatile("" ::: "memory"); } while (0)

typedef __amdgpu_buffer_rsrc_t rsrc_t;
__device__ __forceinline__ rsrc_t mkrs(const void* p, unsigned bytes) { return __builtin_amdgcn_make_buffer_rsrc((void*)p, 0, bytes, 0x00020000); }
__device__ __forceinline__ v4u ldst(rsrc_t rs, unsigned voff, unsigned soff) { return __builtin_amdgcn_raw_buffer_load_b128(rs, voff, soff, AUX_NT); }
#define POLL_LD(dst, voff, base, imm) asm volatile("global_load_dwordx4 %0, %1, %2 offset:" #imm " sc1" : "=v"(dst) : "v"(voff), "s"(base) : "memory")
__device__ __forceinline__ float dot2(unsigned k, unsigned q, float acc) {
  return __builtin_amdgcn_fdot2_f32_bf16(__builtin_bit_cast(bf16x2, k), __builtin_bit_cast(bf16x2, q), acc, false);
}
__device__ __forceinline__ unsigned cvtpk(float lo, float hi) {
  bf16x2 v = {(__bf16)lo, (__bf16)hi};
  return __builtin_bit_cast(unsigned, v);
}
__device__ __forceinline__ float asf(unsigned u) { return __builtin_bit_cast(float, u); }
__device__ __forceinline__ unsigned asu(float f) { return __builtin_bit_cast(unsigned, f); }
__device__ __forceinline__ float bfr(float f) { return asf(cvtpk(f, f) << 16); }
__device__ __forceinline__ float bflo(unsigned u) { return asf(u << 16); }
__device__ __forceinline__ float bfhi(unsigned u) { return asf(u & 0xffff0000u); }
template <int CTRL> __device__ __forceinline__ float dpp(float x) {
  return __builtin_bit_cast(float, __builtin_amdgcn_mov_dpp(__builtin_bit_cast(int, x), CTRL, 0xf, 0xf, true));
}
constexpr int XOR1 = 0xB1, XOR2 = 0x4E, HMIR = 0x141, ROR8 = 0x128, ROR4 = 0x124;
__device__ __forceinline__ float sum8(float x) { x += dpp<XOR1>(x); x += dpp<XOR2>(x); x += dpp<HMIR>(x); return x; }
__device__ __forceinline__ float max8(float x) { x = fmaxf(x, dpp<XOR1>(x)); x = fmaxf(x, dpp<XOR2>(x)); x = fmaxf(x, dpp<HMIR>(x)); return x; }
__device__ __forceinline__ float sum_hi3(float x) {
  x += dpp<ROR8>(x);
  auto s = __builtin_amdgcn_permlane16_swap(asu(x), asu(x), false, false);
  x = asf(s[0]) + asf(s[1]);
  auto t = __builtin_amdgcn_permlane32_swap(asu(x), asu(x), false, false);
  return asf(t[0]) + asf(t[1]);
}
__device__ __forceinline__ float max_hi3(float x) {
  x = fmaxf(x, dpp<ROR8>(x));
  auto s = __builtin_amdgcn_permlane16_swap(asu(x), asu(x), false, false);
  x = fmaxf(asf(s[0]), asf(s[1]));
  auto t = __builtin_amdgcn_permlane32_swap(asu(x), asu(x), false, false);
  return fmaxf(asf(t[0]), asf(t[1]));
}
__device__ __forceinline__ f32x4 mfma(v4u a, v4u b, f32x4 c) {
  return __builtin_amdgcn_mfma_f32_16x16x32_bf16(__builtin_bit_cast(bf16x8, a), __builtin_bit_cast(bf16x8, b), c, 0, 0, 0);
}
__device__ __forceinline__ v4u sel(unsigned pp, const v4u& m) { return v4u{pp & m[0], pp & m[1], pp & m[2], pp & m[3]}; }
template <class T> __device__ __forceinline__ unsigned ldsaddr(T* p) { return (unsigned)(size_t)(LAS T*)p; }
template <int ROW> __device__ __forceinline__ v4u sum_pack(unsigned p, float scale, unsigned tsel) {
  const f32x4 s = (*LP(const f32x4, p) + *LP(const f32x4, p + ROW * 4)) + (*LP(const f32x4, p + ROW * 8) + *LP(const f32x4, p + ROW * 12));
  auto a = __builtin_amdgcn_permlane32_swap(asu(s[0]), asu(s[2]), false, false);
  auto c = __builtin_amdgcn_permlane32_swap(asu(s[1]), asu(s[3]), false, false);
  const float u = asf(a[0]) + asf(a[1]), v = asf(c[0]) + asf(c[1]);
  auto d = __builtin_amdgcn_permlane16_swap(asu(u), asu(u), false, false);
  auto e = __builtin_amdgcn_permlane16_swap(asu(v), asu(v), false, false);
  const unsigned ru = asu((asf(d[0]) + asf(d[1])) * scale) + 0x200u, rv = asu((asf(e[0]) + asf(e[1])) * scale) + 0x200u;
  const unsigned lo = __builtin_amdgcn_alignbit(rv >> 10, ru, 10), hi = __builtin_amdgcn_alignbit(tsel, rv, 20);
  auto f = __builtin_amdgcn_permlane32_swap(lo, lo, false, false);
  auto g = __builtin_amdgcn_permlane32_swap(hi, hi, false, false);
  return v4u{f[0], g[0], f[1], g[1]};
}
__device__ __forceinline__ unsigned ubad(const v4u& g, unsigned twA, unsigned twB) { return ((g[1] ^ twA) | (g[3] ^ twB)) >> 12; }
__device__ __forceinline__ float uval0(unsigned lo) { return asf(lo << 10); }
__device__ __forceinline__ float uval1(unsigned lo, unsigned hi) { return asf(__builtin_amdgcn_alignbit(hi, lo, 12) & 0xfffffc00u); }
__device__ __forceinline__ float sum4_tr(float s0, float s1, float s2, float s3) {
  auto a = __builtin_amdgcn_permlane32_swap(asu(s0), asu(s2), false, false);
  auto c = __builtin_amdgcn_permlane32_swap(asu(s1), asu(s3), false, false);
  auto t = __builtin_amdgcn_permlane16_swap(asu(asf(a[0]) + asf(a[1])), asu(asf(c[0]) + asf(c[1])), false, false);
  float z = asf(t[0]) + asf(t[1]);
  z += dpp<ROR8>(z); z += dpp<ROR4>(z);
  return z;
}
__device__ __forceinline__ bool any_bad(unsigned bad) { return __builtin_amdgcn_ballot_w64(bad != 0u) != 0ull; }
__device__ __forceinline__ float gval(unsigned u) { return asf(u & 0xffffff00u); }

constexpr int I_W2 = 5, I_K = 6, I_V = 30, I_L = 54, I_END = 58, I_DEEP = 5, I_WARM = 19;
constexpr int upto(int i) {
  const int t = i + (i < I_DEEP ? 3 : i == I_DEEP ? 4 : 5);
  return t < I_END - 1 ? t : I_END - 1;
}
constexpr unsigned pace(int i) { return i >= 29 ? 1010 + 285 * (i - 29) / 10 : 270 + 740 * (i - 5) / 24; }
constexpr int RA = 192, RS = 64;
struct alignas(16) Lds {
  float redA[16 * RA];
  float redB[16 * RS];
  float red1[16 * RS];
  float red2[16 * RS];
  unsigned qh[128];
  float wm[8];
  float wl[8];
  u16 xs[640];
};

__global__ void __launch_bounds__(NTHR, 2)
fused_decode_kernel(const u16*, const u16*, const u16*, const u16*, const u16*, const u16*, const u16*, const u16*, const u16*, u16*, unsigned char*) {
  typedef unsigned v16u __attribute__((ext_vector_type(16)));
  v16u ka; v4u kb; u64 kw;
  int b = blockIdx.x;
  asm volatile("s_load_dwordx16 %0, %4, 0x0\n\ts_load_dwordx4 %1, %4, 0x40\n\ts_load_dwordx2 %2, %4, 0x50"
               : "=&s"(ka), "=&s"(kb), "=&s"(kw), "+s"(b) : "s"(__builtin_amdgcn_kernarg_segment_ptr()));
  SB();
  __shared__ Lds L;
  u64 ts64; asm volatile("s_memrealtime %0" : "=s"(ts64));
  bool fast = false;
  asm volatile("" ::: "v147");
  const int tid = threadIdx.x, lane = tid & 63, w = __builtin_amdgcn_readfirstlane(tid >> 6), r = lane >> 3, c = lane & 7;
  const int h = (b & 7) * 4 + ((b >> 3) & 3), j = b >> 5;
  const bool isq = j < 6;
  const int u1 = (j == 6) ? h : 32 + h;
  const bool has1 = isq || u1 < 48;
  const int mq = (j - 6) >> 31;
  int cg = ((3 * h + (j >> 1)) & mq) | ((96 + (u1 >> 1)) & ~mq), half = ((j & mq) | (u1 & ~mq)) & 1;
  unsigned stride = FF * 2 + ((QN * 2 - FF * 2) & mq);
  unsigned o1 = (cg - (96 & ~mq)) * 128, n1 = has1 ? DM * stride : 0u;
  unsigned off1 = (half * 320 + w * 8 + r) * stride + c * 16;
  unsigned kvrow = h * SK + (SK - WIN) + (w * 8 + j) * 8;
  constexpr int BSTR = 512 * DH * 2;
  u64 kvoff = (u64)kvrow * (DH * 2);
  unsigned loff = (unsigned)(r * (DH * 2) + c * 16);
  const int vu = b < 240 ? b : 239;
  int rg = vu / 10, cgrp = vu - rg * 10;
  unsigned o2 = (rg * 64 + w * 8) * (DM * 2) + cgrp * 128;
  const int G = h >> 2, jj = (h & 3) * 8 + j;
  int rgp = jj / 10, cg5 = jj - rgp * 10;
  const bool isred = jj >= 30;
  const int rowb = 256 * rgp + 32 * w;
  unsigned ol = (G * 768 + rowb) * (DM * 2) + cg5 * 128;
  unsigned offl = r * (4 * DM * 2) + c * 16, off2 = r * (DM * 2) + c * 16;
  asm volatile("" : "+s"(kvoff), "+s"(rg), "+s"(cgrp), "+s"(o2), "+s"(rgp), "+s"(cg5), "+s"(ol));
  asm volatile("" : "+v"(loff), "+v"(offl), "+v"(off2));
  unsigned offx = lane * 16, offx2 = (lane & 15) * 16;
  asm volatile("" : "+s"(cg), "+s"(half), "+s"(stride), "+s"(o1), "+s"(n1));
  asm volatile("" : "+v"(off1), "+v"(offx), "+v"(offx2));
  SB();
  asm volatile("s_waitcnt lgkmcnt(0)" : "+s"(ka), "+s"(kb), "+s"(kw), "+s"(ts64));
  const unsigned tstart = (unsigned)ts64;
#define KARG(v, i) ((const unsigned char*)(((u64)(v)[2 * (i) + 1] << 32) | (v)[2 * (i)]))
  const unsigned char *x_ = KARG(ka, 0), *kc_ = KARG(ka, 1), *vc_ = KARG(ka, 2), *wq_ = KARG(ka, 3), *wo_ = KARG(ka, 4), *w1_ = KARG(ka, 5), *b1_ = KARG(ka, 6),
                      *w2_ = KARG(ka, 7), *b2_ = KARG(kb, 0), *out_ = KARG(kb, 1), *ws_ = (const unsigned char*)kw;
  GAS unsigned char* ws = (GAS unsigned char*)ws_;
  const GAS unsigned char* x = (const GAS unsigned char*)x_;
  const rsrc_t rw1 = mkrs((const unsigned char*)(isq ? wq_ : w1_) + o1, n1);
  rsrc_t RW2, RWO, RK, RV;
  const rsrc_t rs = mkrs(ws_, WS_END);
  v4u R[I_END];
  unsigned warm = 0;
#define ISSUE(i) do { if constexpr ((i) < I_W2) R[i] = ldst(rw1, off1, (i) * 64 * stride); \
    else if constexpr ((i) == I_W2) R[i] = ldst(RW2, off2, 0); \
    else if constexpr ((i) >= I_K && (i) < I_K + 24) R[i] = ldst(RK, loff + (((i) - I_K) % 3) * 128, (((i) - I_K) / 3) * BSTR); \
    else if constexpr ((i) >= I_V && (i) < I_V + 24) R[i] = ldst(RV, loff + (((i) - I_V) % 3) * 128, (((i) - I_V) / 3) * BSTR); \
    else if constexpr ((i) >= I_L && (i) < I_L + 4) R[i] = ldst(RWO, offl + ((i) - I_L) * (DM * 2), 0); \
    SB(); } while (0)
#define TOUCH(i) do { asm volatile("" : "+v"(R[i])); SB(); } while (0)
#define THROTTLE(i) do { if constexpr ((i) >= I_DEEP) { if (!fast) asm volatile("s_waitcnt vmcnt(2)" ::: "memory"); SB(); } } while (0)
#define PACE(i) do { if constexpr ((i) >= I_DEEP && (i) < I_END - 6 && ((i) & 1)) { u64 now_; asm volatile("s_memrealtime %0\n\ts_waitcnt lgkmcnt(0)" : "=s"(now_) :: "memory"); \
    fast = (unsigned)now_ - tstart > pace(i); SB(); } } while (0)
#define WARM(i) do { if constexpr ((i) == I_WARM) { warm = __builtin_amdgcn_raw_buffer_load_b32(rs, w < 6 ? WS_OP + (h * 8 + j) * 768 + w * 128 + (lane & 31) * 4 : 0x7ffffff0, 0, AUX_SC1); SB(); } \
    if constexpr ((i) == I_WARM + 5) { asm volatile("" :: "v"(warm)); SB(); } } while (0)
#define STEP(i) do { constexpr int u0_ = upto((i) - 1), u1_ = upto(i); TOUCH(i); PACE(i); \
    if constexpr (u0_ < u1_) { THROTTLE(i); ISSUE(u0_ + 1); } if constexpr (u0_ + 1 < u1_) { THROTTLE(i); ISSUE(u0_ + 2); } WARM(i); } while (0)
#define STEP3(i) do { STEP(i); STEP((i) + 1); STEP((i) + 2); } while (0)

  const v4u xa = *(const GAS v4u*)(x + offx);
  const v4u xb = *(const GAS v4u*)(x + 1024 + offx2);
  SB();
  ISSUE(0); ISSUE(1); ISSUE(2);
  static_assert(upto(-1) == 2 && upto(I_V + 23) == I_END - 1, "issue schedule");
  u64 ep;
  asm volatile("s_load_dwordx2 %0, %1, 0x0" : "=s"(ep) : "s"(ws_) : "memory");
  SB();
  RW2 = mkrs((const unsigned char*)w2_ + o2, b < 240 ? 8 * DM * 2 : 0);
  RWO = mkrs((const unsigned char*)wo_ + ol, isred ? 0 : 32 * DM * 2);
  RK = mkrs((const unsigned char*)kc_ + kvoff, 7 * BSTR + 8 * DH * 2);
  RV = mkrs((const unsigned char*)vc_ + kvoff, 7 * BSTR + 8 * DH * 2);

  v4u msk;
#pragma unroll
  for (int d = 0; d < 4; ++d) msk[d] = ((c >> 1) == d) ? ((c & 1) ? 0xffff0000u : 0x0000ffffu) : 0u;

  STEP(0); STEP(1); STEP(2);
  float mean_b, rden;
  unsigned xr[5];
  {
    const bool hb = lane < 16;
    *(v4u*)&L.xs[lane * 8] = xa;
    if (hb) *(v4u*)&L.xs[512 + lane * 8] = xb;
#pragma unroll
    for (int p = 0; p < 5; ++p) xr[p] = L.xs[half * 320 + p * 64 + w * 8 + r];
    float e[16];
#pragma unroll
    for (int k = 0; k < 4; ++k) { e[2 * k] = bflo(xa[k]); e[2 * k + 1] = bfhi(xa[k]); e[8 + 2 * k] = bflo(xb[k]); e[9 + 2 * k] = bfhi(xb[k]); }
    float s = 0.f, s2 = 0.f;
#pragma unroll
    for (int k = 0; k < 8; ++k) { s += e[k]; s2 += e[8 + k]; }
    s += hb ? s2 : 0.f;
    s = sum_hi3(sum8(s));
    const float mean = s * (1.f / DM);
    float v = 0.f, v2 = 0.f;
#pragma unroll
    for (int k = 0; k < 8; ++k) { const float d0 = e[k] - mean, d1 = e[8 + k] - mean; v = fmaf(d0, d0, v); v2 = fmaf(d1, d1, v2); }
    v += hb ? v2 : 0.f;
    v = sum_hi3(sum8(v));
    mean_b = bfr(mean);
    rden = __builtin_amdgcn_rcpf(bfr(__builtin_amdgcn_sqrtf(bfr(bfr(v * (1.f / DM)) + 1e-5f))));
  }
  SB();
  const bool dvalid = ((lane >> 3) & 1) == (lane >> 5);
  const int drow = w * 2 + (lane >> 5), dpos = c * 8 + ((lane >> 4) & 1) * 4;
  const int l16 = lane & 15;
  {
    v4u a1[5];
#pragma unroll
    for (int p = 0; p < 5; ++p) { const float xn = bfr(bflo(xr[p]) - mean_b) * rden; a1[p] = sel(cvtpk(xn, xn), msk); }
    SB();
    STEP(3); STEP(4);
    f32x4 acc = {0.f, 0.f, 0.f, 0.f};
#pragma unroll
    for (int p = 0; p < 5; ++p) acc = mfma(a1[p], R[p], acc);
    if (dvalid) *(f32x4*)&L.red1[drow * RS + dpos] = acc;
  }
  WG_BARRIER();
  asm volatile("" : "+s"(ep));
  const u64 tg = ep + 1;
  const unsigned tagA = (unsigned)tg, tagH = (unsigned)(tg >> 32), tagB = tagH & 0xffu;
  const unsigned tA20 = tagA & 0xfffffu, tB20 = ((unsigned)(tg >> 20) + tagA * 0x9e5u) & 0xfffffu;
  const unsigned twA = tA20 << 12, twB = tB20 << 12;
  unsigned tsel = lane < 32 ? tA20 : tB20;
  if (w == 7 && has1) {
    const v4u u = sum_pack<RS>(ldsaddr(L.red1) + ((lane >> 4) * (4 * RS) + 4 * l16) * 4, 1.f, tsel);
    if (lane < 16) __builtin_amdgcn_raw_buffer_store_b128(u, rs, WS_S1 + (cg * 2 + half) * 256 + l16 * 16, 0, AUX_SC1);
  }
  SB();
  STEP(5); STEP3(6); STEP3(9); STEP3(12);
  v4u g0, g1; u64 b1p;
  const unsigned goff = WS_S1 + (lane < 48 ? 3 * h + (lane >> 4) : 96 + rg) * 512 + l16 * 16;
  if (w == 0) {
    POLL_LD(g0, goff, ws, 0); POLL_LD(g1, goff, ws, 256);
    asm volatile("global_load_dwordx2 %0, %1, %2" : "=v"(b1p) : "v"(rg * 128 + l16 * 8), "s"(b1_) : "memory");
  }
  SB();
  STEP3(15);
  if (w == 0) {
    asm volatile("s_waitcnt vmcnt(%3)" : "+v"(g0), "+v"(g1), "+v"(b1p) : "n"(upto(17) - upto(14)) : "memory");
    unsigned spins = 0;
    while (any_bad(ubad(g0, twA, twB) | ubad(g1, twA, twB))) {
      if (++spins > SPIN_CAP) break;
      __builtin_amdgcn_s_sleep(1);
      asm volatile("global_load_dwordx4 %0, %2, %3 sc1\n\tglobal_load_dwordx4 %1, %2, %3 offset:256 sc1\n\ts_waitcnt vmcnt(0)"
                   : "=&v"(g0), "=&v"(g1) : "v"(goff), "s"(ws) : "memory");
    }
    float a[4] = {uval0(g0[0]) + uval0(g1[0]), uval1(g0[0], g0[1]) + uval1(g1[0], g1[1]), uval0(g0[2]) + uval0(g1[2]), uval1(g0[2], g0[3]) + uval1(g1[2], g1[3])};
    if (lane >= 48) {
      const unsigned bl = (unsigned)b1p, bh = (unsigned)(b1p >> 32);
      const float bb[4] = {bflo(bl), bfhi(bl), bflo(bh), bfhi(bh)};
#pragma unroll
      for (int k = 0; k < 4; ++k) { const float t = fmaxf(bfr(bfr(a[k]) + bb[k]), 0.f); a[k] = t * t; }
    }
    *(u64*)&L.qh[2 * lane] = ((u64)cvtpk(a[2], a[3]) << 32) | cvtpk(a[0], a[1]);
  }
  WG_BARRIER();
  float sc[8];
  {
    v4u q[3];
#pragma unroll
    for (int i = 0; i < 3; ++i) q[i] = *(const v4u*)&L.qh[i * 32 + c * 4];
    const unsigned hv = ((const u16*)&L.qh[96])[w * 8 + r];
    constexpr float SCALE = 0.07216878364870322f, L2E = 1.4426950408889634f;
#define QK(bk) do { float d_ = 0.f; _Pragma("unroll") for (int i = 0; i < 3; ++i) _Pragma("unroll") for (int k = 0; k < 4; ++k) d_ = dot2(R[I_K + 3 * (bk) + i][k], q[i][k], d_); \
    sc[bk] = bfr(bfr(sum8(d_)) * SCALE) * L2E; SB(); } while (0)
#define W2MM() do { const f32x4 acc_ = mfma(sel(hv | (hv << 16), msk), R[I_W2], f32x4{0.f, 0.f, 0.f, 0.f}); if (dvalid) *(f32x4*)&L.red2[drow * RS + dpos] = acc_; SB(); } while (0)
    STEP(18); QK(0); STEP(19); QK(1); STEP(20); QK(2); STEP(21); QK(3); STEP(22); QK(4); STEP(23); QK(5);
    STEP(24); W2MM(); STEP(25); STEP(26); QK(6); STEP3(27); QK(7);
  }
  {
    float m = sc[0];
#pragma unroll
    for (int bk = 1; bk < 8; ++bk) m = fmaxf(m, sc[bk]);
    m = max_hi3(m);
    float l = 0.f;
#pragma unroll
    for (int bk = 0; bk < 8; ++bk) l += __builtin_amdgcn_exp2f(sc[bk] - m);
    l = sum_hi3(l);
    if (lane == 0) { L.wm[w] = m; L.wl[w] = l; }
  }
  SB();
  STEP(I_V);
  WG_BARRIER();
  const float mw = L.wm[c], M = max8(mw);
  if (w == 5) {
    const float l = sum8(__builtin_amdgcn_exp2f(mw - M) * L.wl[c]);
    if (lane < 8) __builtin_amdgcn_raw_buffer_store_b128(v4u{asu(M), tagA, (asu(l) & 0xffffff00u) | tagB, tagA}, rs, WS_ML + (h * 8 + j) * 128 + lane * 16, 0, AUX_SC1);
  }
  SB();
  v4u A[8];
#pragma unroll
  for (int bk = 0; bk < 8; ++bk) { const float p = __builtin_amdgcn_exp2f(sc[bk] - M); A[bk] = sel(cvtpk(p, p), msk); }
  SB();
  STEP(I_V + 1);
  if (w == 7 && b < 240) {
    const v4u u = sum_pack<RS>(ldsaddr(L.red2) + ((lane >> 4) * (4 * RS) + 4 * l16) * 4, 1.f, tsel);
    if (lane < 16) __builtin_amdgcn_raw_buffer_store_b128(u, rs, WS_FIN + (cgrp * 2 + (l16 >> 3)) * FIN_STRIDE + (24 + rg) * 128 + (l16 & 7) * 16, 0, AUX_SC1);
  }
  SB();
  float F;
  const int prw = w * 16 + l16;
  unsigned aA = ldsaddr(L.redA) + ((lane >> 4) * (4 * RA) + 4 * prw) * 4, aAw = ldsaddr(L.redA) + (drow * RA + dpos) * 4;
  unsigned aB = ldsaddr(L.redB) + ((lane >> 4) * (4 * RS) + 4 * l16) * 4, aBw = ldsaddr(L.redB) + (drow * RS + dpos) * 4;
  unsigned sA = WS_OP + (h * 8 + j) * 768 + prw * 16;
  unsigned sB = WS_FIN + (cg5 * 2 + (l16 >> 3)) * FIN_STRIDE + (G * 3 + rgp) * 128 + (l16 & 7) * 16;
  const int hq = rowb / DH;
  unsigned ooff = WS_OP + ((4 * G + hq) * 8 + c) * 768 + (rowb - hq * DH) * 4 + r * 16;
  const v4u z4 = {0u, 0u, 0u, 0u};
  {
    v4u gm;
    const unsigned moff = WS_ML + (h * 8 + c) * 128 + r * 16;
    constexpr int NAFTER = upto(I_V + 20) - upto(I_V + 17);
    f32x4 acc[3] = {{0.f, 0.f, 0.f, 0.f}, {0.f, 0.f, 0.f, 0.f}, {0.f, 0.f, 0.f, 0.f}};
#define PV(i) do { acc[(i) % 3] = mfma(A[(i) / 3], R[I_V + (i)], acc[(i) % 3]); SB(); } while (0)
#define SPV(i) do { STEP(I_V + (i)); PV(i); } while (0)
#define SPV3(i) do { SPV(i); SPV((i) + 1); SPV((i) + 2); } while (0)
    PV(0); PV(1); SPV(2); SPV3(3); SPV3(6); SPV3(9); SPV3(12); SPV3(15);
    POLL_LD(gm, moff, ws, 0);
    SB();
    SPV3(18);
    asm volatile("s_waitcnt vmcnt(%1)" : "+v"(gm) : "n"(NAFTER) : "memory");
    {
      unsigned spins = 0;
      while (any_bad((gm[1] ^ tagA) | (gm[3] ^ tagA) | ((gm[2] ^ tagB) & 0xffu))) {
        if (++spins > SPIN_CAP) break;
        __builtin_amdgcn_s_sleep(2);
        asm volatile("global_load_dwordx4 %0, %1, %2 sc1\n\ts_waitcnt vmcnt(0)" : "=&v"(gm) : "v"(moff), "s"(ws) : "memory");
      }
      const float mc = asf(gm[0]), Mh = max8(mc);
      F = __builtin_amdgcn_exp2f(M - Mh) * __builtin_amdgcn_rcpf(sum8(__builtin_amdgcn_exp2f(mc - Mh) * gval(gm[2])));
    }
    asm volatile("" : "+v"(F), "+v"(aA), "+v"(aAw), "+v"(sA), "+v"(ooff), "+v"(aB), "+v"(aBw), "+v"(sB), "+v"(tsel));
    SB();
    SPV(21); SPV(22);
    if (dvalid) { *LP(f32x4, aAw) = acc[0]; *LP(f32x4, aAw + 256) = acc[1]; }
    SB();
    SPV(23);
    if (dvalid) *LP(f32x4, aAw + 512) = acc[2];
  }
  WG_BARRIER();
  if (w < 3) {
    const v4u u = sum_pack<RA>(aA, F, tsel);
    if (lane < 16) __builtin_amdgcn_raw_buffer_store_b128(u, rs, sA, 0, AUX_SC1);
  }
  SB();
  if (!isred) {
    u64 ga_, gb_;
    unsigned spins = SPIN_CAP;
    u64 sv;
    asm volatile(
      "s_mov_b64 %[sv], exec\n\t"
      "1:\n\t"
      "global_load_dwordx4 v[128:131], %[off], %[base] sc1\n\t"
      "s_waitcnt vmcnt(0)\n\t"
      "v_xor_b32 v144, %[ta], v129\n\tv_xor_b32 v145, %[tb], v131\n\tv_or_b32 v144, v144, v145\n\tv_cmp_lt_u32 vcc, 0xfff, v144\n\t"
      "s_and_b64 exec, exec, vcc\n\t"
      "s_cbranch_scc0 2f\n\t"
      "s_sleep 1\n\t"
      "s_sub_u32 %[n], %[n], 1\n\ts_cbranch_scc0 1b\n\t"
      "2:\n\t"
      "s_mov_b64 exec, %[sv]\n\t"
      "v_mov_b64 %[a], v[128:129]\n\tv_mov_b64 %[b], v[130:131]"
      : [a] "=&v"(ga_), [b] "=&v"(gb_), [n] "+s"(spins), [sv] "=&s"(sv)
      : [off] "v"(ooff), [base] "s"(ws), [ta] "s"(twA), [tb] "s"(twB)
      : "memory", "vcc", "scc", "v128", "v129", "v130", "v131", "v144", "v145");
    const float o4[4] = {sum8(uval0((unsigned)ga_)), sum8(uval1((unsigned)ga_, (unsigned)(ga_ >> 32))), sum8(uval0((unsigned)gb_)), sum8(uval1((unsigned)gb_, (unsigned)(gb_ >> 32)))};
    f32x4 acc = {0.f, 0.f, 0.f, 0.f};
#pragma unroll
    for (int p = 0; p < 4; ++p) acc = mfma(sel(cvtpk(o4[p], o4[p]), msk), R[I_L + p], acc);
    if (dvalid) *LP(f32x4, aBw) = acc;
    WG_BARRIER();
    if (w == 0) {
      const v4u u = sum_pack<RS>(aB, 1.f, tsel);
      if (lane < 16) __builtin_amdgcn_raw_buffer_store_b128(u, rs, sB, 0, AUX_SC1);
    }
  } else if (G * 2 + (jj - 30) + 16 * w < 40) {
    const int fl = G * 2 + (jj - 30) + 16 * w, col = fl * 16 + (lane & 3) * 4 + (lane >> 4);
    const unsigned xv = *(const GAS u16*)(x + col * 2), b2v = *(const GAS u16*)((const GAS unsigned char*)b2_ + col * 2);
    const unsigned foff = WS_FIN + (fl >> 1) * FIN_STRIDE + (lane >> 2) * 128 + (fl & 1) * 64 + (lane & 3) * 16, foff2 = foff + (lane < 32 ? 2048 : 1024);
    v4u m0, m1;
    unsigned spins = 0;
    for (;;) {
      asm volatile("global_load_dwordx4 %0, %2, %3 offset:3072 sc1\n\tglobal_load_dwordx4 %1, %4, %3 offset:3072 sc1\n\ts_waitcnt vmcnt(0)"
                   : "=&v"(m0), "=&v"(m1) : "v"(foff), "s"(ws), "v"(foff2) : "memory");
      if (!any_bad(ubad(m0, twA, twB) | ubad(m1, twA, twB)) || ++spins > SPIN_CAP) break;
      __builtin_amdgcn_s_sleep(1);
    }
    if (lane >= 32) m1 = z4;
    const float O = sum4_tr(uval0(m0[0]) + uval0(m1[0]), uval1(m0[0], m0[1]) + uval1(m1[0], m1[1]), uval0(m0[2]) + uval0(m1[2]), uval1(m0[2], m0[3]) + uval1(m1[2], m1[3]));
    const float t0 = bfr(bfr(O) + bflo(b2v));
    float ga = bfr(t0 * __builtin_amdgcn_rcpf(1.f + __builtin_amdgcn_exp2f(-1.4426950408889634f * t0)));
    GAS u16* op = (GAS u16*)((GAS unsigned char*)out_ + col * 2);
    float x0 = bflo(xv);
    asm volatile("" : "+v"(ga), "+v"(op), "+v"(x0));
    u64 ua[2], ub[2];
    spins = SPIN_CAP;
    asm volatile(
      "global_load_dwordx4 v[128:131], %[off], %[base] sc1\n\tglobal_load_dwordx4 v[132:135], %[off2], %[base] sc1\n\t" "s_sleep 5\n\t"
      "1:\n\t"
      "global_load_dwordx4 v[136:139], %[off], %[base] sc1\n\tglobal_load_dwordx4 v[140:143], %[off2], %[base] sc1\n\t" "s_waitcnt vmcnt(2)\n\t"
      "v_xor_b32 v144, %[ta], v129\n\tv_xor_b32 v145, %[tb], v131\n\tv_xor_b32 v146, %[ta], v133\n\tv_xor_b32 v147, %[tb], v135\n\t" "v_or3_b32 v144, v144, v145, v146\n\tv_or_b32 v144, v144, v147\n\tv_cmp_lt_u32 vcc, 0xfff, v144\n\t" "s_cbranch_vccz 2f\n\t"
      "global_load_dwordx4 v[128:131], %[off], %[base] sc1\n\tglobal_load_dwordx4 v[132:135], %[off2], %[base] sc1\n\t" "s_waitcnt vmcnt(2)\n\t"
      "v_xor_b32 v144, %[ta], v137\n\tv_xor_b32 v145, %[tb], v139\n\tv_xor_b32 v146, %[ta], v141\n\tv_xor_b32 v147, %[tb], v143\n\t" "v_or3_b32 v144, v144, v145, v146\n\tv_or_b32 v144, v144, v147\n\tv_cmp_lt_u32 vcc, 0xfff, v144\n\t" "s_cbranch_vccz 3f\n\t"
      "s_sub_u32 %[n], %[n], 1\n\ts_cbranch_scc0 1b\n\t"
      "3:\n\t" "v_mov_b64 %[a0], v[136:137]\n\tv_mov_b64 %[b0], v[138:139]\n\t" "v_mov_b64 %[a1], v[140:141]\n\tv_mov_b64 %[b1], v[142:143]\n\t" "s_branch 5f\n\t"
      "2:\n\t" "v_mov_b64 %[a0], v[128:129]\n\tv_mov_b64 %[b0], v[130:131]\n\t" "v_mov_b64 %[a1], v[132:133]\n\tv_mov_b64 %[b1], v[134:135]\n\t"
      "5:"
      : [a0] "=&v"(ua[0]), [b0] "=&v"(ub[0]), [a1] "=&v"(ua[1]), [b1] "=&v"(ub[1]), [n] "+s"(spins)
      : [off] "v"(foff), [off2] "v"(foff2), [base] "s"(ws), [ta] "s"(twA), [tb] "s"(twB)
      : "memory", "vcc", "scc", "v128", "v129", "v130", "v131", "v132", "v133", "v134", "v135", "v136", "v137", "v138", "v139", "v140", "v141", "v142", "v143", "v144", "v145", "v146", "v147");
    if (lane >= 32) { ua[1] = 0; ub[1] = 0; }
    float s4[4] = {0.f, 0.f, 0.f, 0.f};
#pragma unroll
    for (int i = 0; i < 2; ++i) {
      const unsigned al = (unsigned)ua[i], ah = (unsigned)(ua[i] >> 32), bl = (unsigned)ub[i], bh = (unsigned)(ub[i] >> 32);
      s4[0] += uval0(al); s4[1] += uval1(al, ah); s4[2] += uval0(bl); s4[3] += uval1(bl, bh);
    }
    const float S = sum4_tr(s4[0], s4[1], s4[2], s4[3]);
    const float y = x0 + bfr(bfr(S) * ga);
    if (!(lane & 12)) *op = (u16)cvtpk(y, y);
    if (fl == 0 && lane == 0) __hip_atomic_store((GAS u64*)(ws + WS_EPOCH), ((u64)tagH << 32) | tagA, __ATOMIC_RELAXED, __HIP_MEMORY_SCOPE_AGENT);
  }
}

extern "C" void kernel_launch(void* const* d_in, const int* in_sizes, int n_in, void* d_out, int out_size, void* d_ws, size_t ws_size,
                              hipStream_t stream) {
  (void)in_sizes; (void)n_in; (void)out_size; (void)ws_size;
  fused_decode_kernel<<<NWG, NTHR, 0, stream>>>((const u16*)d_in[0], (const u16*)d_in[1], (const u16*)d_in[2], (const u16*)d_in[3],
                                                (const u16*)d_in[4], (const u16*)d_in[6], (const u16*)d_in[7], (const u16*)d_in[8],
                                                (const u16*)d_in[9], (u16*)d_out, (unsigned char*)d_ws);
}
```
